```python
import torch

def _softmax(x, axis=-1):
    return torch.softmax(x, dim=axis)
def _gelu(x):
    return torch.nn.functional.gelu(x, approximate="tanh")
def _silu(x):
    return torch.nn.functional.silu(x)
def _sigmoid(x):
    return torch.sigmoid(x)
def _layernorm(x, axis=-1, eps=1e-5):
    m = x.mean(dim=axis, keepdim=True)
    v = x.var(dim=axis, keepdim=True, unbiased=False)
    return (x - m) / torch.sqrt(v + eps)
def _scatter_add(tgt, idx, src, axis=0):
    return tgt.clone().index_add_(axis, idx, src)
def _segment_sum(src, seg, n):
    out = torch.zeros((n,) + src.shape[1:], dtype=src.dtype)
    return out.index_add_(0, seg, src)
def _pad(x, pw):
    flat = tuple(v for pair in reversed(pw) for v in pair)
    return torch.nn.functional.pad(x, flat)

kernel_name = 'parallel_sum__grouped_kv_mqa__swiglu_ffn__prompt'

def setup_inputs():

    _w = (lambda a: (lambda t: t.to(torch.bfloat16) if t.is_floating_point() else t)(torch.as_tensor(a)))
    def _A_setup():
        B, S, D, H = 1, 2048, 640, 32
        Dh = D // H
        return {
            "x": _w((lambda *s: torch.randn(*s, dtype=torch.bfloat16) * 0.05)(B, S, D)),
            "wq": _w((lambda *s: torch.randn(*s, dtype=torch.bfloat16) * 0.2)(D, H * Dh)),
            "wk": _w((lambda *s: torch.randn(*s, dtype=torch.bfloat16) * 0.2)(D, Dh)),
            "wv": _w((lambda *s: torch.randn(*s, dtype=torch.bfloat16) * 0.1)(D, Dh)),
            "wo": _w((lambda *s: torch.randn(*s, dtype=torch.bfloat16) * 0.1)(H * Dh, D)),
        }
    _A_in = _A_setup()

    def _B_setup():
        B, S, D, F = 1, 2048, 640, 1536
        return {
            "x": _w((lambda *s: torch.randn(*s, dtype=torch.bfloat16) * 0.05)(B, S, D)),
            "w_gate": _w((lambda *s: torch.randn(*s, dtype=torch.bfloat16) * 0.1)(D, F)),
            "w_up": _w((lambda *s: torch.randn(*s, dtype=torch.bfloat16) * 0.1)(D, F)),
            "w_down": _w((lambda *s: torch.randn(*s, dtype=torch.bfloat16) * 0.1)(F, D)),
        }
    _B_in = _B_setup()

    return {
        "A_x": _A_in["x"],
        "A_wq": _A_in["wq"],
        "A_wk": _A_in["wk"],
        "A_wv": _A_in["wv"],
        "A_wo": _A_in["wo"],
        "B_x": _B_in["x"],
        "B_w_gate": _B_in["w_gate"],
        "B_w_up": _B_in["w_up"],
        "B_w_down": _B_in["w_down"],
    }

def reference(A_x, A_wq, A_wk, A_wv, A_wo, B_x, B_w_gate, B_w_up, B_w_down):
    def _A_ref(x, wq, wk, wv, wo):
        B, S, D = x.shape
        H = 32
        Dh = D // H
        q = (lambda x, *d: x.permute(*d))((x @ wq).reshape(B, S, H, Dh), 0, 2, 1, 3)
        k = x @ wk
        v = x @ wv
        scores = torch.einsum("bhqd,bkd->bhqk", q, k) * (Dh ** -0.5)
        p = _softmax(scores, axis=-1)
        o = torch.einsum("bhqk,bkd->bhqd", p, v)
        return (lambda x, *d: x.permute(*d))(o, 0, 2, 1, 3).reshape(B, S, H * Dh) @ wo

    def _B_ref(x, w_gate, w_up, w_down):
        g = _silu(x @ w_gate)
        u = x @ w_up
        return (g * u) @ w_down

    x = A_x
    xn = _layernorm(x, axis=-1)
    return x + _A_ref(xn, A_wq, A_wk, A_wv, A_wo) + _B_ref(xn, B_w_gate, B_w_up, B_w_down)
```

```cpp
#include <hip/hip_runtime.h>
#include <hip/hip_bf16.h>
typedef __hip_bfloat16 bf16;
#define S 2048
#define D 640
#define H 32
#define DH 20
#define F 1536
#define MIMIC 0
__device__ __forceinline__ float b2f(bf16 x){ return __bfloat162float(x); }
__device__ __forceinline__ float rb(float x){ return MIMIC ? __bfloat162float(__float2bfloat16(x)) : x; }

__global__ void k_ln(const bf16* x, float* xn){
  int r = blockIdx.x; int t = threadIdx.x;
  float s = 0; for (int i = t; i < D; i += 64) s += b2f(x[r*D+i]);
  for (int o = 32; o; o >>= 1) s += __shfl_xor(s, o);
  float m = s / D;
  float v = 0; for (int i = t; i < D; i += 64) { float d = b2f(x[r*D+i]) - m; v += d*d; }
  for (int o = 32; o; o >>= 1) v += __shfl_xor(v, o);
  v /= D;
  float mr = rb(m), vr = rb(v);
  float sd = rb(sqrtf(rb(vr + 1e-5f)));
  for (int i = t; i < D; i += 64) xn[r*D+i] = rb(rb(b2f(x[r*D+i]) - mr) / sd);
}
__global__ void k_gemm(const float* A, const bf16* B, float* C, int M, int N, int K, int ldc){
  int n = blockIdx.x * 64 + threadIdx.x; int m = blockIdx.y;
  if (n >= N) return;
  float acc = 0;
  for (int k = 0; k < K; ++k) acc += A[m*K+k] * b2f(B[k*N+n]);
  C[m*ldc+n] = rb(acc);
}
__global__ void k_attn(const float* q, const float* k, const float* v, float* o){
  int idx = blockIdx.x * 64 + threadIdx.x; int h = idx % H; int r = idx / H;
  float qq[DH]; for (int d = 0; d < DH; ++d) qq[d] = q[r*D + h*DH + d];
  const float sc = MIMIC ? rb(0.22360679775f) : 0.22360679775f;
  float m = -1e30f;
  for (int j = 0; j < S; ++j) { float s = 0; for (int d = 0; d < DH; ++d) s += qq[d]*k[j*DH+d]; s = rb(rb(s) * 0.22360679775f); m = fmaxf(m, s); }
  float l = 0;
  for (int j = 0; j < S; ++j) { float s = 0; for (int d = 0; d < DH; ++d) s += qq[d]*k[j*DH+d]; s = rb(rb(s) * 0.22360679775f); l += __expf(s - m); }
  float acc[DH]; for (int d = 0; d < DH; ++d) acc[d] = 0;
  for (int j = 0; j < S; ++j) { float s = 0; for (int d = 0; d < DH; ++d) s += qq[d]*k[j*DH+d]; s = rb(rb(s) * 0.22360679775f); float p = rb(__expf(s - m) / l); for (int d = 0; d < DH; ++d) acc[d] += p * v[j*DH+d]; }
  for (int d = 0; d < DH; ++d) o[r*D + h*DH + d] = rb(acc[d]);
  (void)sc;
}
__global__ void k_glu(const float* g, const float* u, float* h){
  int i = blockIdx.x * 256 + threadIdx.x;
  float x = g[i]; float s = rb(x / (1.f + __expf(-x)));
  h[i] = rb(s * u[i]);
}
__global__ void k_fin(const bf16* x, const float* a, const float* b, bf16* out){
  int i = blockIdx.x * 256 + threadIdx.x;
  out[i] = __float2bfloat16(rb(b2f(x[i]) + a[i]) + b[i]);
}
extern "C" void kernel_launch(void* const* d_in, const int* in_sizes, int n_in, void* d_out, int out_size, void* d_ws, size_t ws_size, hipStream_t stream) {
  const bf16* x = (const bf16*)d_in[0]; const bf16* wq = (const bf16*)d_in[1]; const bf16* wk = (const bf16*)d_in[2]; const bf16* wv = (const bf16*)d_in[3]; const bf16* wo = (const bf16*)d_in[4];
  const bf16* wg = (const bf16*)d_in[6]; const bf16* wu = (const bf16*)d_in[7]; const bf16* wd = (const bf16*)d_in[8];
  float* ws = (float*)d_ws;
  float* xn = ws; float* q = xn + S*D; float* k = q + S*D; float* v = k + S*DH; float* o = v + S*DH; float* ao = o + S*D; float* g = ao + S*D; float* u = g + S*F; float* hh = u + S*F; float* bo = hh + S*F;
  k_ln<<<S, 64, 0, stream>>>(x, xn);
  k_gemm<<<dim3(D/64, S), 64, 0, stream>>>(xn, wq, q, S, D, D, D);
  k_gemm<<<dim3(1, S), 64, 0, stream>>>(xn, wk, k, S, DH, D, DH);
  k_gemm<<<dim3(1, S), 64, 0, stream>>>(xn, wv, v, S, DH, D, DH);
  k_attn<<<S*H/64, 64, 0, stream>>>(q, k, v, o);
  k_gemm<<<dim3(D/64, S), 64, 0, stream>>>(o, wo, ao, S, D, D, D);
  k_gemm<<<dim3(F/64, S), 64, 0, stream>>>(xn, wg, g, S, F, D, F);
  k_gemm<<<dim3(F/64, S), 64, 0, stream>>>(xn, wu, u, S, F, D, F);
  k_glu<<<S*F/256, 256, 0, stream>>>(g, u, hh);
  k_gemm<<<dim3(D/64, S), 64, 0, stream>>>(hh, wd, bo, S, D, F, D);
  k_fin<<<S*D/256, 256, 0, stream>>>(x, ao, bo, (bf16*)d_out);
}
```

```cpp
#include <hip/hip_runtime.h>
#include <hip/hip_bf16.h>
#include <stdint.h>

typedef float f32x2 __attribute__((ext_vector_type(2)));
typedef float f32x4 __attribute__((ext_vector_type(4)));
typedef float f32x16 __attribute__((ext_vector_type(16)));
typedef __bf16 bf16x2 __attribute__((ext_vector_type(2)));
typedef __bf16 bf16x8 __attribute__((ext_vector_type(8)));
typedef short s16x4 __attribute__((ext_vector_type(4)));
typedef unsigned u32x2 __attribute__((ext_vector_type(2)));
typedef unsigned u32x4 __attribute__((ext_vector_type(4)));
#define LAS __attribute__((address_space(3)))
#define DI __device__ __forceinline__
typedef LAS char* lds_ptr;

constexpr int SEQ = 2048, DM = 640, NH = 32, DH = 20, FF = 1536;
constexpr size_t WS_XN = 0;
constexpr size_t WS_KV = WS_XN + 128 * 20 * 1024;
constexpr size_t WS_Q  = WS_KV + 16 * 12288;
constexpr size_t WS_H  = WS_Q + 32 * 64 * 1280;
constexpr size_t WS_O  = WS_H + 128 * 48 * 1024;
constexpr size_t WS_END = WS_O + 128 * 20 * 1024;
constexpr int KV_STAGE = 12288;

DI unsigned cvtpk(float a, float b) { f32x2 v = {a, b}; bf16x2 r = __builtin_convertvector(v, bf16x2); return __builtin_bit_cast(unsigned, r); }
DI float bf_round(float a) { return __uint_as_float(cvtpk(a, 0.f) << 16); }
DI float bf_lo(unsigned u) { return __uint_as_float(u << 16); }
DI float bf_hi(unsigned u) { return __uint_as_float(u & 0xffff0000u); }
DI void glds16(const void* g, lds_ptr l) { __builtin_amdgcn_global_load_lds((const unsigned*)g, (LAS unsigned*)l, 16, 0, 0); }
DI s16x4 tr_read(lds_ptr p) { return __builtin_bit_cast(s16x4, __builtin_amdgcn_ds_read_tr16_b64_v4i16((LAS s16x4*)p)); }
DI bf16x8 cat8(s16x4 a, s16x4 b) { return __builtin_bit_cast(bf16x8, __builtin_shufflevector(a, b, 0, 1, 2, 3, 4, 5, 6, 7)); }
DI bf16x8 lds_frag(lds_ptr p) { return __builtin_bit_cast(bf16x8, *(LAS u32x4*)p); }
#define MFMA16(a, b, c) __builtin_amdgcn_mfma_f32_16x16x32_bf16((a), (b), (c), 0, 0, 0)
#define MFMA32(a, b, c) __builtin_amdgcn_mfma_f32_32x32x16_bf16((a), (b), (c), 0, 0, 0)
#define MFMA32T(a, b, c) __builtin_amdgcn_mfma_f32_32x32x8bf16_1k((a), (b), (c), 0, 0, 0)
#define WAIT_V(n) asm volatile("s_waitcnt vmcnt(" #n ")" ::: "memory")
#define WAIT_L(n) asm volatile("s_waitcnt lgkmcnt(" #n ")" ::: "memory")
#define BAR() __builtin_amdgcn_s_barrier()
#define SBAR() __builtin_amdgcn_sched_barrier(0)
template <int CTRL> DI float dpp_add(float x) { return x + __uint_as_float(__builtin_amdgcn_update_dpp(0u, __float_as_uint(x), CTRL, 0xf, 0xf, true)); }
DI float row16_sum(float x) { x = dpp_add<0x128>(x); x = dpp_add<0x124>(x); x = dpp_add<0x122>(x); x = dpp_add<0x121>(x); return x; }

constexpr int P1_LDS = 20480 + 51200 + 64;
__global__ __launch_bounds__(256) void k_ln_kv(const uint16_t* __restrict__ x, const uint16_t* __restrict__ wk, const uint16_t* __restrict__ wv, char* __restrict__ ws) {
  extern __shared__ __attribute__((aligned(16))) char smem_[];
  lds_ptr smem = (lds_ptr)smem_;
  const int tid = threadIdx.x, lane = tid & 63, wid = __builtin_amdgcn_readfirstlane(tid >> 6), b = blockIdx.x;
  lds_ptr s_xn = smem, s_w = smem + 20480;
  const int rr = 4 * wid + (lane >> 4), c = lane & 15;
  const uint16_t* xrow = x + (size_t)(16 * b + rr) * DM;
  u32x4 xv[5];
#pragma unroll
  for (int i = 0; i < 5; ++i) xv[i] = *(const u32x4*)(xrow + (c + 16 * i) * 8);
  for (int i = wid; i < 50; i += 4) glds16((i < 25 ? wk + i * 512 : wv + (i - 25) * 512) + lane * 8, s_w + i * 1024);
  float f[40];
#pragma unroll
  for (int i = 0; i < 5; ++i)
#pragma unroll
    for (int j = 0; j < 4; ++j) { f[8 * i + 2 * j] = bf_lo(xv[i][j]); f[8 * i + 2 * j + 1] = bf_hi(xv[i][j]); }
  float s = 0.f;
#pragma unroll
  for (int i = 0; i < 40; ++i) s += f[i];
  const float mean = row16_sum(s) * (1.0f / DM);
  float v = 0.f;
#pragma unroll
  for (int i = 0; i < 40; ++i) { const float d = f[i] - mean; v = fmaf(d, d, v); }
  const float var = row16_sum(v) * (1.0f / DM);
  const float mr = bf_round(mean);
  const float sd = bf_round(__builtin_sqrtf(bf_round(bf_round(var) + 1e-5f)));
  const float rinv = 1.0f / sd;
#pragma unroll
  for (int i = 0; i < 5; ++i) {
    u32x4 o;
#pragma unroll
    for (int j = 0; j < 4; ++j) {
      const unsigned d2 = cvtpk(f[8 * i + 2 * j] - mr, f[8 * i + 2 * j + 1] - mr);
      o[j] = cvtpk(bf_lo(d2) * rinv, bf_hi(d2) * rinv);
    }
    *(LAS u32x4*)(s_xn + (4 * i + (c >> 2)) * 1024 + (rr + 16 * (c & 3)) * 16) = o;
  }
  WAIT_V(0);
  __syncthreads();
  {
    char* g = ws + WS_XN + (size_t)b * 20480;
#pragma unroll
    for (int i = 0; i < 5; ++i) *(u32x4*)(g + (i * 256 + tid) * 16) = *(LAS u32x4*)(s_xn + (i * 256 + tid) * 16);
  }
  const int sel = wid >> 1, t = wid & 1;
  const int g = lane >> 4, i16 = lane & 15, q = i16 >> 2, p = i16 & 3;
  lds_ptr wbase = s_w + sel * 25600 + (8 * g + q) * 40 + 32 * t + 8 * p;
  f32x4 acc = {0.f, 0.f, 0.f, 0.f};
#pragma unroll
  for (int ks = 0; ks < 20; ++ks) {
    const bf16x8 a = lds_frag(s_xn + ks * 1024 + lane * 16);
    const bf16x8 w = cat8(tr_read(wbase + ks * 32 * 40), tr_read(wbase + ks * 32 * 40 + 4 * 40));
    if (sel == 0) acc = MFMA16(w, a, acc); else acc = MFMA16(a, w, acc);
  }
  const u32x2 pk = {cvtpk(acc[0], acc[1]), cvtpk(acc[2], acc[3])};
  if (sel == 0) {
    const int kb = b >> 1, key = (b & 1) * 16 + i16;
    char* blk = ws + WS_KV + (size_t)(kb >> 2) * KV_STAGE + (kb & 3) * 1280;
    if (t == 0) *(u32x2*)(blk + (key + 32 * (g & 1)) * 16 + (g >> 1) * 8) = pk;
    else if (g == 0) *(u32x2*)(blk + 1024 + key * 8) = pk;
  } else {
    const int d = 16 * t + i16;
    char* blk = ws + WS_KV + (size_t)(b >> 3) * KV_STAGE + 5120 + (b & 7) * 896 + (g & 1) * 448 + d * 16 + (g >> 1) * 8;
    if (d < 20) *(u32x2*)blk = pk;
    else if (d < 28) { const unsigned cst = (d == 20) ? 0x3f803f80u : 0u; *(u32x2*)blk = (u32x2){cst, cst}; }
  }
}

constexpr int G1_STAGE = 8192 + 16384, G1_SLOTS = 3, G1_LDS = G1_STAGE * G1_SLOTS;
constexpr float QSCALE = 0.22360679774997896f * 1.4426950408889634f;
__global__ __launch_bounds__(256, 2) void k_gemm1(const uint16_t* __restrict__ wq, const uint16_t* __restrict__ wg, const uint16_t* __restrict__ wu, char* __restrict__ ws) {
  extern __shared__ __attribute__((aligned(16))) char smem_[];
  lds_ptr smem = (lds_ptr)smem_;
  const int tid = threadIdx.x, lane = tid & 63, wid = __builtin_amdgcn_readfirstlane(tid >> 6);
  const int b = blockIdx.x;
  const int rb = b & 15, cg = b >> 4;
  const bool isq = cg >= 12;
  const uint16_t* wsrc[4]; int ldw;
  if (!isq) { ldw = FF; wsrc[0] = wg + cg * 128; wsrc[1] = wg + cg * 128 + 64; wsrc[2] = wu + cg * 128; wsrc[3] = wu + cg * 128 + 64; }
  else { ldw = DM; const int c0 = (cg - 12) * 256; wsrc[0] = wq + c0; wsrc[1] = wq + c0 + 64; wsrc[2] = wq + (c0 + 128 < DM ? c0 + 128 : c0); wsrc[3] = wq + (c0 + 192 < DM ? c0 + 192 : c0 + 64); }
  const char* asrc = ws + WS_XN + (size_t)(rb * 8 + 2 * wid) * 20480 + lane * 16;
  const int kr = lane >> 3, ch = lane & 7;
  const uint16_t* wl[4];
#pragma unroll
  for (int i = 0; i < 4; ++i) { const int k = 8 * i + kr; const int f = ((k >> 1) & 1) | (((k >> 3) & 1) << 1);
    const uint16_t* pw = wid == 0 ? wsrc[0] : wid == 1 ? wsrc[1] : wid == 2 ? wsrc[2] : wsrc[3];
    wl[i] = pw + (size_t)k * ldw + (((ch >> 1) ^ f) * 16 + (ch & 1) * 8); }
  auto stage = [&](int ks, int slot) {
    lds_ptr sb = smem + slot * G1_STAGE;
    glds16(asrc + ks * 1024, sb + (2 * wid) * 1024);
    glds16(asrc + 20480 + ks * 1024, sb + (2 * wid + 1) * 1024);
#pragma unroll
    for (int i = 0; i < 4; ++i) glds16(wl[i] + (size_t)ks * 32 * ldw, sb + 8192 + wid * 4096 + i * 1024);
  };
  const int wp = wid & 1, wn = wid >> 1;
  const int g = lane >> 4, i16 = lane & 15, q = i16 >> 2, p = i16 & 3;
  const int fsw = ((q >> 1) & 1) | ((g & 1) << 1);
  int woff[4];
#pragma unroll
  for (int t = 0; t < 4; ++t) woff[t] = 8192 + (8 * g + q) * 128 + ((t ^ fsw) * 32) + 8 * p;
  f32x4 acc[4][8];
#pragma unroll
  for (int i = 0; i < 4; ++i)
#pragma unroll
    for (int j = 0; j < 8; ++j) acc[i][j] = (f32x4){0.f, 0.f, 0.f, 0.f};
  stage(0, 0); stage(1, 1);
  int slot = 0;
  for (int ks = 0; ks < 20; ++ks) {
    if (ks < 19) WAIT_V(6); else WAIT_V(0);
    BAR();
    if (ks + 2 < 20) stage(ks + 2, slot == 0 ? 2 : slot - 1);
    lds_ptr sb = smem + slot * G1_STAGE;
    bf16x8 a[4];
#pragma unroll
    for (int i = 0; i < 4; ++i) a[i] = lds_frag(sb + (4 * wp + i) * 1024 + lane * 16);
#pragma unroll
    for (int j = 0; j < 8; ++j) {
      const int panel = wn + 2 * (j >> 2);
      lds_ptr wp_ = sb + woff[j & 3] + panel * 4096;
      const bf16x8 w = cat8(tr_read(wp_), tr_read(wp_ + 512));
#pragma unroll
      for (int i = 0; i < 4; ++i) acc[i][j] = MFMA16(w, a[i], acc[i][j]);
    }
    slot = slot == 2 ? 0 : slot + 1;
  }
  if (!isq) {
#pragma unroll
    for (int i = 0; i < 4; ++i) {
      const int rt = rb * 8 + 4 * wp + i;
#pragma unroll
      for (int j = 0; j < 4; ++j) {
        float h[4];
#pragma unroll
        for (int r = 0; r < 4; ++r) {
          const float gv = bf_round(acc[i][j][r]), uv = bf_round(acc[i][j + 4][r]);
          const float sg = bf_round(gv * __builtin_amdgcn_rcpf(1.f + __builtin_amdgcn_exp2f(-1.4426950408889634f * gv)));
          h[r] = sg * uv;
        }
        const int n = cg * 128 + wn * 64 + j * 16 + 4 * g;
        char* dst = ws + WS_H + ((size_t)rt * 48 + (n >> 5)) * 1024 + (i16 + 16 * ((n >> 3) & 3)) * 16 + ((n >> 2) & 1) * 8;
        *(u32x2*)dst = (u32x2){cvtpk(h[0], h[1]), cvtpk(h[2], h[3])};
      }
    }
  } else {
#pragma unroll
    for (int i = 0; i < 4; ++i) {
      const int pos = (rb * 8 + 4 * wp + i) * 16 + i16;
#pragma unroll
      for (int j = 0; j < 8; ++j) {
        const int c0 = (cg - 12) * 256 + (wn + 2 * (j >> 2)) * 64;
        if (c0 < DM) {
          const int n = c0 + (j & 3) * 16 + 4 * g;
          const int head = n / 20, qd = (n - head * 20) >> 2;
          char* blk = ws + WS_Q + ((size_t)head * 64 + (pos >> 5)) * 1280;
          char* dst = qd < 4 ? blk + ((pos & 31) + 32 * (qd & 1)) * 16 + (qd >> 1) * 8 : blk + 1024 + (pos & 31) * 8;
          *(u32x2*)dst = (u32x2){cvtpk(acc[i][j][0] * QSCALE, acc[i][j][1] * QSCALE), cvtpk(acc[i][j][2] * QSCALE, acc[i][j][3] * QSCALE)};
        }
      }
    }
  }
}

constexpr int AT_SLOTS = 3, AT_LDS = AT_SLOTS * KV_STAGE + 64;
constexpr float MARGIN = 96.f;
DI float xhalf_max(float m) { auto r = __builtin_amdgcn_permlane32_swap(__float_as_uint(m), __float_as_uint(m), false, false); return fmaxf(__uint_as_float(r[0]), __uint_as_float(r[1])); }
DI float max16(const f32x16& s) {
  float a = fmaxf(fmaxf(s[0], s[1]), s[2]), b = fmaxf(fmaxf(s[3], s[4]), s[5]);
  a = fmaxf(fmaxf(a, s[6]), s[7]); b = fmaxf(fmaxf(b, s[8]), s[9]); a = fmaxf(fmaxf(a, s[10]), s[11]); b = fmaxf(fmaxf(b, s[12]), s[13]);
  return fmaxf(fmaxf(a, s[14]), fmaxf(b, s[15]));
}
DI f32x16 splat16(float v) { f32x16 r;
#pragma unroll
  for (int i = 0; i < 16; ++i) r[i] = v;
  return r; }

__global__ __launch_bounds__(256, 2) void k_attn(char* __restrict__ ws) {
  extern __shared__ __attribute__((aligned(16))) char smem_[];
  lds_ptr smem = (lds_ptr)smem_;
  const int tid = threadIdx.x, lane = tid & 63, wid = __builtin_amdgcn_readfirstlane(tid >> 6);
  const int b = blockIdx.x, pb = b >> 3, hg = b & 7;
  const int head = hg * 4 + wid, r32 = lane & 31, hi = lane >> 5;
  const char* kvsrc = ws + WS_KV + wid * 3072 + lane * 16;
  auto stage = [&](int t, int slot) {
#pragma unroll
    for (int i = 0; i < 3; ++i) glds16(kvsrc + (size_t)t * KV_STAGE + i * 1024, smem + slot * KV_STAGE + wid * 3072 + i * 1024);
  };
  stage(0, 0); stage(1, 1); stage(2, 2);
  const char* qsrc = ws + WS_Q + ((size_t)head * 64 + pb * 2) * 1280;
  bf16x8 qm[2]; s16x4 qt[2];
#pragma unroll
  for (int u = 0; u < 2; ++u) {
    qm[u] = __builtin_bit_cast(bf16x8, *(const u32x4*)(qsrc + u * 1280 + lane * 16));
    u32x2 tq = *(const u32x2*)(qsrc + u * 1280 + 1024 + r32 * 8);
    if (hi) tq = (u32x2){0u, 0u};
    qt[u] = __builtin_bit_cast(s16x4, tq);
  }
  const int koff = lane * 16, ktoff = 1024 + r32 * 8, voff = 5120 + hi * 448 + (r32 < 28 ? r32 : 27) * 16;
  LAS int* flag = (LAS int*)(smem + AT_SLOTS * KV_STAGE);
  if (tid == 0) *flag = 0;
  WAIT_V(0);
  __syncthreads();
  f32x16 negm[2];
  {
#pragma unroll
    for (int u = 0; u < 2; ++u) {
      float m = -3.0e38f;
#pragma unroll
      for (int kb = 0; kb < 2; ++kb) {
        f32x16 s = MFMA32(lds_frag(smem + kb * 1280 + koff), qm[u], splat16(0.f));
        s = MFMA32T(*(LAS s16x4*)(smem + kb * 1280 + ktoff), qt[u], s);
        m = fmaxf(m, max16(s));
      }
      negm[u] = splat16(-(xhalf_max(m) + MARGIN));
    }
  }
  f32x16 o[2];
  for (int pass = 0;; ++pass) {
    o[0] = splat16(0.f); o[1] = splat16(0.f);
    int slot = 0;
    for (int t = 0; t < 16; ++t) {
      lds_ptr sb = smem + slot * KV_STAGE;
#pragma unroll
      for (int kb = 0; kb < 4; ++kb) {
        const bf16x8 km = lds_frag(sb + kb * 1280 + koff);
        const s16x4 kt = *(LAS s16x4*)(sb + kb * 1280 + ktoff);
        const bf16x8 v0 = lds_frag(sb + voff + (2 * kb) * 896), v1 = lds_frag(sb + voff + (2 * kb + 1) * 896);
#pragma unroll
        for (int u = 0; u < 2; ++u) {
          f32x16 s = MFMA32(km, qm[u], negm[u]);
          s = MFMA32T(kt, qt[u], s);
          u32x4 p0, p1;
#pragma unroll
          for (int i = 0; i < 4; ++i) {
            p0[i] = cvtpk(__builtin_amdgcn_exp2f(s[2 * i]), __builtin_amdgcn_exp2f(s[2 * i + 1]));
            p1[i] = cvtpk(__builtin_amdgcn_exp2f(s[8 + 2 * i]), __builtin_amdgcn_exp2f(s[8 + 2 * i + 1]));
          }
          o[u] = MFMA32(v0, __builtin_bit_cast(bf16x8, p0), o[u]);
          o[u] = MFMA32(v1, __builtin_bit_cast(bf16x8, p1), o[u]);
        }
      }
      if (t + 1 < 16) {
        if (t + 2 < 16) WAIT_V(3); else WAIT_V(0);
        WAIT_L(0);
        BAR();
        if (t + 3 < 16) stage(t + 3, slot);
      }
      slot = slot == 2 ? 0 : slot + 1;
    }
    bool bad = false;
#pragma unroll
    for (int u = 0; u < 2; ++u) {
      if (hi) bad |= !(o[u][8] > 0.f && o[u][8] < 1.0e36f);
#pragma unroll
      for (int r = 0; r < 12; ++r) bad |= !(fabsf(o[u][r]) < 3.0e38f);
    }
    if (pass == 0 && __any(bad) && lane == 0) *flag = 1;
    __syncthreads();
    if (pass == 1 || *flag == 0) break;
    float m[2] = {-3.0e38f, -3.0e38f};
    for (int t = 0; t < 16; ++t) {
      __syncthreads();
      stage(t, 0);
      WAIT_V(0);
      __syncthreads();
#pragma unroll
      for (int kb = 0; kb < 4; ++kb)
#pragma unroll
        for (int u = 0; u < 2; ++u) {
          f32x16 s = MFMA32(lds_frag(smem + kb * 1280 + koff), qm[u], splat16(0.f));
          s = MFMA32T(*(LAS s16x4*)(smem + kb * 1280 + ktoff), qt[u], s);
          m[u] = fmaxf(m[u], max16(s));
        }
    }
    negm[0] = splat16(-xhalf_max(m[0])); negm[1] = splat16(-xhalf_max(m[1]));
    __syncthreads();
    stage(0, 0); stage(1, 1); stage(2, 2);
    WAIT_V(0);
    __syncthreads();
  }
#pragma unroll
  for (int u = 0; u < 2; ++u) {
    auto sw = __builtin_amdgcn_permlane32_swap(__float_as_uint(o[u][8]), __float_as_uint(o[u][8]), false, false);
    const float l = __uint_as_float(sw[1]);
    const float rl = 1.0f / l;
    const int pos = pb * 64 + u * 32 + r32;
    char* rowb = ws + WS_O + (size_t)(pos >> 4) * 20480 + (pos & 15) * 16;
#pragma unroll
    for (int qq = 0; qq < 3; ++qq) {
      const int qd = 2 * qq + hi;
      if (qd < 5) {
        const int cq = head * 5 + qd;
        const int c8 = cq >> 1;
        char* dst = rowb + (c8 >> 2) * 1024 + (c8 & 3) * 256 + (cq & 1) * 8;
        *(u32x2*)dst = (u32x2){cvtpk(o[u][4 * qq] * rl, o[u][4 * qq + 1] * rl), cvtpk(o[u][4 * qq + 2] * rl, o[u][4 * qq + 3] * rl)};
      }
    }
  }
}

constexpr int G2_STAGE = 12288 + 8192, G2_SLOTS = 3, G2_LDS = G2_STAGE * G2_SLOTS;
__global__ __launch_bounds__(256, 2) void k_gemm2(const uint16_t* __restrict__ x, const uint16_t* __restrict__ wo, const uint16_t* __restrict__ wd, const char* __restrict__ ws, uint16_t* __restrict__ out) {
  extern __shared__ __attribute__((aligned(16))) char smem_[];
  lds_ptr smem = (lds_ptr)smem_;
  const int tid = threadIdx.x, lane = tid & 63, wid = __builtin_amdgcn_readfirstlane(tid >> 6);
  const int b = blockIdx.x, pn = b % 10, rb = b / 10;
  const int rt0 = rb * 6;
  const int kr = lane >> 3, ch = lane & 7;
  int aoffH[3], aoffO[3];
#pragma unroll
  for (int i = 0; i < 3; ++i) { const int id = 3 * wid + i, pt = id >> 1, kk = id & 1; int rt = rt0 + pt; rt = rt < 128 ? rt : 127;
    aoffH[i] = (rt * 48 + kk) * 1024 + lane * 16; aoffO[i] = (rt * 20 + kk) * 1024 + lane * 16; }
  int woff_g[2];
#pragma unroll
  for (int i = 0; i < 2; ++i) { const int k = 8 * (2 * wid + i) + kr; const int f = ((k >> 1) & 1) | (((k >> 3) & 1) << 1);
    woff_g[i] = k * DM + pn * 64 + (((ch >> 1) ^ f) * 16 + (ch & 1) * 8); }
  auto stage = [&](int st, int slot) {
    lds_ptr sb = smem + slot * G2_STAGE;
    const bool isH = st < 24;
    const char* ab = isH ? ws + WS_H + st * 2048 : ws + WS_O + (st - 24) * 2048;
    const uint16_t* wb = isH ? wd + (size_t)st * 64 * DM : wo + (size_t)(st - 24) * 64 * DM;
#pragma unroll
    for (int i = 0; i < 3; ++i) glds16(ab + (isH ? aoffH[i] : aoffO[i]), sb + (3 * wid + i) * 1024);
#pragma unroll
    for (int i = 0; i < 2; ++i) glds16(wb + woff_g[i], sb + 12288 + (2 * wid + i) * 1024);
  };
  const int wp = wid & 1, wn = wid >> 1;
  const int g = lane >> 4, i16 = lane & 15, q = i16 >> 2, p = i16 & 3;
  const int fsw = ((q >> 1) & 1) | ((g & 1) << 1);
  int woff[2];
#pragma unroll
  for (int t = 0; t < 2; ++t) woff[t] = 12288 + (8 * g + q) * 128 + (((2 * wn + t) ^ fsw) * 32) + 8 * p;
  f32x4 acc[3][2], accH[3][2];
#pragma unroll
  for (int i = 0; i < 3; ++i)
#pragma unroll
    for (int j = 0; j < 2; ++j) acc[i][j] = (f32x4){0.f, 0.f, 0.f, 0.f};
  stage(0, 0); stage(1, 1);
  int slot = 0;
  for (int st = 0; st < 34; ++st) {
    if (st < 33) WAIT_V(5); else WAIT_V(0);
    BAR();
    if (st + 2 < 34) stage(st + 2, slot == 0 ? 2 : slot - 1);
    lds_ptr sb = smem + slot * G2_STAGE;
    if (st == 24) {
#pragma unroll
      for (int i = 0; i < 3; ++i)
#pragma unroll
        for (int j = 0; j < 2; ++j) { accH[i][j] = acc[i][j]; acc[i][j] = (f32x4){0.f, 0.f, 0.f, 0.f}; }
    }
#pragma unroll
    for (int kk = 0; kk < 2; ++kk) {
      bf16x8 a[3], w[2];
#pragma unroll
      for (int i = 0; i < 3; ++i) a[i] = lds_frag(sb + ((3 * wp + i) * 2 + kk) * 1024 + lane * 16);
#pragma unroll
      for (int j = 0; j < 2; ++j) w[j] = cat8(tr_read(sb + woff[j] + kk * 4096), tr_read(sb + woff[j] + kk * 4096 + 512));
#pragma unroll
      for (int i = 0; i < 3; ++i)
#pragma unroll
        for (int j = 0; j < 2; ++j) acc[i][j] = MFMA16(w[j], a[i], acc[i][j]);
    }
    slot = slot == 2 ? 0 : slot + 1;
  }
#pragma unroll
  for (int i = 0; i < 3; ++i) {
    const int rt = rt0 + 3 * wp + i;
    if (rt < 128) {
      const int pos = rt * 16 + i16;
#pragma unroll
      for (int j = 0; j < 2; ++j) {
        const int n = pn * 64 + (2 * wn + j) * 16 + 4 * g;
        const u32x2 xv = *(const u32x2*)(x + (size_t)pos * DM + n);
        const float xf[4] = {bf_lo(xv[0]), bf_hi(xv[0]), bf_lo(xv[1]), bf_hi(xv[1])};
        float r[4];
#pragma unroll
        for (int e = 0; e < 4; ++e) r[e] = bf_round(xf[e] + bf_round(acc[i][j][e])) + bf_round(accH[i][j][e]);
        *(u32x2*)(out + (size_t)pos * DM + n) = (u32x2){cvtpk(r[0], r[1]), cvtpk(r[2], r[3])};
      }
    }
  }
}

extern "C" void kernel_launch(void* const* d_in, const int* in_sizes, int n_in, void* d_out, int out_size, void* d_ws, size_t ws_size, hipStream_t stream) {
  const uint16_t* x = (const uint16_t*)d_in[0];
  const uint16_t* wq = (const uint16_t*)d_in[1];
  const uint16_t* wk = (const uint16_t*)d_in[2];
  const uint16_t* wv = (const uint16_t*)d_in[3];
  const uint16_t* wo = (const uint16_t*)d_in[4];
  const uint16_t* wg = (const uint16_t*)d_in[6];
  const uint16_t* wu = (const uint16_t*)d_in[7];
  const uint16_t* wd = (const uint16_t*)d_in[8];
  char* ws = (char*)d_ws;
  (void)hipFuncSetAttribute((const void*)k_ln_kv, hipFuncAttributeMaxDynamicSharedMemorySize, P1_LDS);
  (void)hipFuncSetAttribute((const void*)k_gemm1, hipFuncAttributeMaxDynamicSharedMemorySize, G1_LDS);
  (void)hipFuncSetAttribute((const void*)k_attn, hipFuncAttributeMaxDynamicSharedMemorySize, AT_LDS);
  (void)hipFuncSetAttribute((const void*)k_gemm2, hipFuncAttributeMaxDynamicSharedMemorySize, G2_LDS);
  k_ln_kv<<<128, 256, P1_LDS, stream>>>(x, wk, wv, ws);
  k_gemm1<<<240, 256, G1_LDS, stream>>>(wq, wg, wu, ws);
  k_attn<<<256, 256, AT_LDS, stream>>>(ws);
  k_gemm2<<<220, 256, G2_LDS, stream>>>(x, wo, wd, ws, (uint16_t*)d_out);
}
```

```cpp
#include <hip/hip_runtime.h>
#include <hip/hip_bf16.h>
#include <stdint.h>

typedef float f32x2 __attribute__((ext_vector_type(2)));
typedef float f32x4 __attribute__((ext_vector_type(4)));
typedef float f32x16 __attribute__((ext_vector_type(16)));
typedef __bf16 bf16x2 __attribute__((ext_vector_type(2)));
typedef __bf16 bf16x8 __attribute__((ext_vector_type(8)));
typedef short s16x4 __attribute__((ext_vector_type(4)));
typedef unsigned u32x2 __attribute__((ext_vector_type(2)));
typedef unsigned u32x4 __attribute__((ext_vector_type(4)));
#define LAS __attribute__((address_space(3)))
#define DI __device__ __forceinline__
typedef LAS char* lds_ptr;
#define GAS __attribute__((address_space(1)))
typedef GAS unsigned gu32;
typedef GAS unsigned long long gu64;
#define RLX_AGENT __ATOMIC_RELAXED, __HIP_MEMORY_SCOPE_AGENT

constexpr int SEQ = 2048, DM = 640, NH = 32, DH = 20, FF = 1536;
constexpr size_t WS_XN = 0;
constexpr size_t WS_KV = WS_XN + 128 * 20 * 1024;
constexpr size_t WS_Q  = WS_KV + 16 * 12288;
constexpr size_t WS_H  = WS_Q + 32 * 64 * 1280;
constexpr size_t WS_O  = WS_H + 128 * 48 * 1024;
constexpr size_t WS_Y  = WS_O + 128 * 20 * 1024;
constexpr size_t WS_CNT = WS_Y + 128 * 40 * 512;
constexpr size_t WS_CNTO = WS_CNT + 4096;
constexpr size_t WS_END = WS_CNTO + 8192;
constexpr int KV_STAGE = 12288;
#define TSTAMP(kid, idx) do {} while (0)

DI unsigned cvtpk(float a, float b) { f32x2 v = {a, b}; bf16x2 r = __builtin_convertvector(v, bf16x2); return __builtin_bit_cast(unsigned, r); }
DI float bf_round(float a) { return __uint_as_float(cvtpk(a, 0.f) << 16); }
DI float bf_lo(unsigned u) { return __uint_as_float(u << 16); }
DI float bf_hi(unsigned u) { return __uint_as_float(u & 0xffff0000u); }
DI void glds16(const void* g, lds_ptr l) { __builtin_amdgcn_global_load_lds((const unsigned*)g, (LAS unsigned*)l, 16, 0, 0); }
DI void dma16(const void* sbase, unsigned voff, unsigned lds) {
  asm volatile("s_mov_b32 m0, %2\n\ts_nop 0\n\tglobal_load_lds_dwordx4 %0, %1" :: "v"(voff), "s"(sbase), "s"(lds) : "memory");
}
DI void dma16x3(const void* sbase, unsigned voff, unsigned lds) {
  asm volatile("s_mov_b32 m0, %2\n\ts_nop 0\n\tglobal_load_lds_dwordx4 %0, %1\n\tglobal_load_lds_dwordx4 %0, %1 offset:1024\n\tglobal_load_lds_dwordx4 %0, %1 offset:2048" :: "v"(voff), "s"(sbase), "s"(lds) : "memory");
}
DI unsigned lds_addr(lds_ptr p) { return (unsigned)(size_t)p; }
#define PIN(x) asm volatile("" : "+v"(x))
DI void m0set(unsigned lds) { asm volatile("s_mov_b32 m0, %0\n\ts_nop 0" :: "s"(lds) : "memory"); }
template <int OFF> DI void dmaoff(const void* sbase, unsigned voff) { asm volatile("global_load_lds_dwordx4 %0, %1 offset:%2" :: "v"(voff), "s"(sbase), "i"(OFF) : "memory"); }
#define WAIT_LGKM0() __builtin_amdgcn_s_waitcnt(0xC07F)
DI s16x4 tr_read(lds_ptr p) { return __builtin_bit_cast(s16x4, __builtin_amdgcn_ds_read_tr16_b64_v4i16((LAS s16x4*)p)); }
DI bf16x8 cat8(s16x4 a, s16x4 b) { return __builtin_bit_cast(bf16x8, __builtin_shufflevector(a, b, 0, 1, 2, 3, 4, 5, 6, 7)); }
DI bf16x8 lds_frag(lds_ptr p) { return __builtin_bit_cast(bf16x8, *(LAS u32x4*)p); }
#define MFMA16(a, b, c) __builtin_amdgcn_mfma_f32_16x16x32_bf16((a), (b), (c), 0, 0, 0)
#define MFMA32(a, b, c) __builtin_amdgcn_mfma_f32_32x32x16_bf16((a), (b), (c), 0, 0, 0)
#define MFMA32T(a, b, c) __builtin_amdgcn_mfma_f32_32x32x8bf16_1k((a), (b), (c), 0, 0, 0)
#define WAIT_V(n) asm volatile("s_waitcnt vmcnt(" #n ")" ::: "memory")
#define WAIT_L(n) asm volatile("s_waitcnt lgkmcnt(" #n ")" ::: "memory")
#define BAR() __builtin_amdgcn_s_barrier()
#define SBAR() __builtin_amdgcn_sched_barrier(0)
template <int CTRL> DI float dpp_add(float x) { return x + __uint_as_float(__builtin_amdgcn_update_dpp(0u, __float_as_uint(x), CTRL, 0xf, 0xf, true)); }
DI float row16_sum(float x) { x = dpp_add<0x128>(x); x = dpp_add<0x124>(x); x = dpp_add<0x122>(x); x = dpp_add<0x121>(x); return x; }

DI float xhalf_max(float m) { auto r = __builtin_amdgcn_permlane32_swap(__float_as_uint(m), __float_as_uint(m), false, false); return fmaxf(__uint_as_float(r[0]), __uint_as_float(r[1])); }
DI float max16(const f32x16& s) {
  float a = fmaxf(fmaxf(s[0], s[1]), s[2]), b = fmaxf(fmaxf(s[3], s[4]), s[5]);
  a = fmaxf(fmaxf(a, s[6]), s[7]); b = fmaxf(fmaxf(b, s[8]), s[9]); a = fmaxf(fmaxf(a, s[10]), s[11]); b = fmaxf(fmaxf(b, s[12]), s[13]);
  return fmaxf(fmaxf(a, s[14]), fmaxf(b, s[15]));
}
DI f32x16 splat16(float v) { f32x16 r;
#pragma unroll
  for (int i = 0; i < 16; ++i) r[i] = v;
  return r; }
#define EX2(x) __builtin_amdgcn_exp2f(x)
#define BF8(x) __builtin_bit_cast(bf16x8, x)

constexpr float QSCALE = 0.22360679774997896f * 1.4426950408889634f;
constexpr int PRE_WKV = 81920, PRE_RING = PRE_WKV + 25600 + 64, PRE_STAGE = 16384, PRE_LDS = PRE_RING + 3 * PRE_STAGE;
__global__ __launch_bounds__(256) void k_pre(const uint16_t* __restrict__ x, const uint16_t* __restrict__ wq, const uint16_t* __restrict__ wk, const uint16_t* __restrict__ wv, char* __restrict__ ws) {
  extern __shared__ __attribute__((aligned(16))) char smem_[];
  lds_ptr smem = (lds_ptr)smem_;
  const int tid = threadIdx.x, lane = tid & 63, wid = __builtin_amdgcn_readfirstlane(tid >> 6), b = blockIdx.x;
  const int pb = (b & 7) + 8 * ((b >> 3) & 3), hg = b >> 5;
  char* ws_dbg = ws; (void)ws_dbg; TSTAMP(0, 0);
  if (b == 0 && tid < 48) *(unsigned*)(ws + WS_CNT + tid * 256) = 0u;
  const int c = lane & 15, rq = lane >> 4;
  const uint16_t* xrow = x + (size_t)(pb * 64 + wid * 16 + rq) * DM + c * 8;
  u32x4 xv[4][5];
#pragma unroll
  for (int ps = 0; ps < 4; ++ps)
#pragma unroll
    for (int i = 0; i < 5; ++i) xv[ps][i] = *(const u32x4*)(xrow + (size_t)ps * 4 * DM + i * 128);
  const unsigned lds0 = lds_addr(smem);
  const int c0 = hg * 80, win = c0 & ~63, coff = c0 - win;
  const int kr = lane >> 3, ch = lane & 7;
  unsigned wl[4];
#pragma unroll
  for (int i = 0; i < 4; ++i) { const int k = 32 * (wid & 1) + 8 * i + kr;
    wl[i] = (unsigned)(k * DM + (wid >> 1) * 64 + (((ch >> 2) ^ ((k >> 1) & 1)) * 32 + (ch & 3) * 8)) * 2u - 1024u * i; }
  const uint16_t* wqw = wq + win;
  auto stage = [&](int st, int slot) {
    const uint16_t* s_ = wqw + (size_t)st * 64 * DM;
    m0set(lds0 + PRE_RING + slot * PRE_STAGE + wid * 4096);
    dmaoff<0>(s_, wl[0]); dmaoff<1024>(s_, wl[1]); dmaoff<2048>(s_, wl[2]); dmaoff<3072>(s_, wl[3]);
  };
  { const uint16_t* wsel = (hg & 1) ? wv : wk;
    for (int i = wid; i < 25; i += 4) dma16(wsel + i * 512, lane * 16, lds0 + PRE_WKV + i * 1024); }
  stage(0, 0); stage(1, 1); stage(2, 2);
#pragma unroll
  for (int ps = 0; ps < 4; ++ps) {
    float f[40];
#pragma unroll
    for (int i = 0; i < 5; ++i)
#pragma unroll
      for (int j = 0; j < 4; ++j) { f[8 * i + 2 * j] = bf_lo(xv[ps][i][j]); f[8 * i + 2 * j + 1] = bf_hi(xv[ps][i][j]); }
    float s = 0.f;
#pragma unroll
    for (int i = 0; i < 40; ++i) s += f[i];
    const float mean = row16_sum(s) * (1.0f / DM);
    float v = 0.f;
#pragma unroll
    for (int i = 0; i < 40; ++i) { const float d = f[i] - mean; v = fmaf(d, d, v); }
    const float var = row16_sum(v) * (1.0f / DM);
    const float mr = bf_round(mean);
    const float sd = bf_round(__builtin_sqrtf(bf_round(bf_round(var) + 1e-5f)));
    const float rinv = 1.0f / sd;
    const int r16 = 4 * ps + rq;
    const bool pub = (wid == (hg >> 1)) && ((ps >> 1) == (hg & 1));
    char* gx = ws + WS_XN + (size_t)(pb * 4 + wid) * 20480;
#pragma unroll
    for (int i = 0; i < 5; ++i) {
      u32x4 o;
#pragma unroll
      for (int j = 0; j < 4; ++j) {
        const unsigned d2 = cvtpk(f[8 * i + 2 * j] - mr, f[8 * i + 2 * j + 1] - mr);
        o[j] = cvtpk(bf_lo(d2) * rinv, bf_hi(d2) * rinv);
      }
      const int off = (4 * i + (c >> 2)) * 1024 + (r16 + 16 * (c & 3)) * 16;
      *(LAS u32x4*)(smem + wid * 20480 + off) = o;
      if (pub) *(u32x4*)(gx + off) = o;
    }
  }
  WAIT_V(0); WAIT_L(0); BAR();
  TSTAMP(0, 1);
  const int g = lane >> 4, i16 = lane & 15, q = i16 >> 2, p = i16 & 3;
  if (wid < 2) {
    const int t = wid, sel = hg & 1, rtg = pb * 4 + (hg >> 1);
    lds_ptr ab = smem + (hg >> 1) * 20480 + lane * 16;
    lds_ptr wbase = smem + PRE_WKV + (8 * g + q) * 40 + 32 * t + 8 * p;
    f32x4 acc = {0.f, 0.f, 0.f, 0.f};
#pragma unroll
    for (int ks = 0; ks < 20; ++ks) {
      const bf16x8 a = lds_frag(ab + ks * 1024);
      const bf16x8 w = cat8(tr_read(wbase + ks * 32 * 40), tr_read(wbase + ks * 32 * 40 + 4 * 40));
      if (sel == 0) acc = MFMA16(w, a, acc); else acc = MFMA16(a, w, acc);
    }
    const u32x2 pk = {cvtpk(acc[0], acc[1]), cvtpk(acc[2], acc[3])};
    if (sel == 0) {
      const int kb = rtg >> 1, key = (rtg & 1) * 16 + i16;
      char* blk = ws + WS_KV + (size_t)(kb >> 2) * KV_STAGE + (kb & 3) * 1280;
      if (t == 0) *(u32x2*)(blk + (key + 32 * (g & 1)) * 16 + (g >> 1) * 8) = pk;
      else if (g == 0) *(u32x2*)(blk + 1024 + key * 8) = pk;
    } else {
      const int d = 16 * t + i16;
      char* blk = ws + WS_KV + (size_t)(rtg >> 3) * KV_STAGE + 5120 + (rtg & 7) * 896 + (g & 1) * 448 + d * 16 + (g >> 1) * 8;
      if (d < 20) *(u32x2*)blk = pk;
      else if (d < 28) { const unsigned cst = (d == 20) ? 0x3f803f80u : 0u; *(u32x2*)blk = (u32x2){cst, cst}; }
    }
  }
  const int r32 = lane & 31, hi = lane >> 5, nh = (lane >> 4) & 1;
  const int col = coff + 20 * wid + 16 * nh + 4 * p;
  const int wa = PRE_RING + ((col >> 6) & 1) * 8192 + (8 * hi + q) * 128 + ((((col >> 5) & 1) ^ ((q >> 1) & 1)) * 64) + (col & 31) * 2;
  const int xa = (r32 >> 4) * 20480 + ((r32 & 15) + 16 * hi) * 16;
  f32x16 q0 = splat16(0.f), q1 = splat16(0.f);
  int slot = 0;
  for (int st = 0; st < 10; ++st) {
    if (st) { if (st < 8) WAIT_V(8); else if (st == 8) WAIT_V(4); else WAIT_V(0); BAR(); }
    lds_ptr wb = smem + slot * PRE_STAGE + wa;
    lds_ptr xb = smem + xa + st * 2048;
    bf16x8 w[4], a0[4], a1[4];
#pragma unroll
    for (int k4 = 0; k4 < 4; ++k4) {
      w[k4] = cat8(tr_read(wb + k4 * 2048), tr_read(wb + k4 * 2048 + 512));
      a0[k4] = lds_frag(xb + (k4 >> 1) * 1024 + (k4 & 1) * 512);
      a1[k4] = lds_frag(xb + 40960 + (k4 >> 1) * 1024 + (k4 & 1) * 512);
    }
    WAIT_LGKM0();
    BAR();
    if (st + 3 < 10) stage(st + 3, slot);
#pragma unroll
    for (int k4 = 0; k4 < 4; ++k4) { q0 = MFMA32(w[k4], a0[k4], q0); q1 = MFMA32(w[k4], a1[k4], q1); }
    slot = slot == 2 ? 0 : slot + 1;
  }
  TSTAMP(0, 2);
  {
    const int head = hg * 4 + wid;
    char* qb = ws + WS_Q + ((size_t)head * 64 + pb * 2) * 1280;
#pragma unroll
    for (int u = 0; u < 2; ++u) {
      const f32x16& qq = u ? q1 : q0;
      *(u32x4*)(qb + u * 1280 + lane * 16) = (u32x4){cvtpk(qq[0] * QSCALE, qq[1] * QSCALE), cvtpk(qq[2] * QSCALE, qq[3] * QSCALE), cvtpk(qq[4] * QSCALE, qq[5] * QSCALE), cvtpk(qq[6] * QSCALE, qq[7] * QSCALE)};
      if (!hi) *(u32x2*)(qb + u * 1280 + 1024 + r32 * 8) = (u32x2){cvtpk(qq[8] * QSCALE, qq[9] * QSCALE), cvtpk(qq[10] * QSCALE, qq[11] * QSCALE)};
    }
  }
  TSTAMP(0, 3);
}

constexpr int AT_SLOTS = 4, AT_LDS = AT_SLOTS * KV_STAGE + 64;
constexpr float MARGIN = 96.f;
DI void attn_body(char* __restrict__ ws, lds_ptr smem, const int b) {
  const int tid = threadIdx.x, lane = tid & 63, wid = __builtin_amdgcn_readfirstlane(tid >> 6);
  const int pb = b >> 3, hg = b & 7;
  const int head = hg * 4 + wid, r32 = lane & 31, hi = lane >> 5;
  char* ws_dbg = ws; (void)ws_dbg; TSTAMP(2, 0);
  const char* kvg = ws + WS_KV + wid * 3072;
  const unsigned ldsw = lds_addr(smem) + wid * 3072, voffl = lane * 16;
#define AT_STAGE(t) dma16x3(kvg + (size_t)(t) * KV_STAGE, voffl, ldsw + ((t) & 3) * KV_STAGE)
  const char* qsrc = ws + WS_Q + ((size_t)head * 64 + pb * 2) * 1280;
  bf16x8 qm0, qm1; s16x4 qt0, qt1;
  {
    u32x4 a0 = *(const u32x4*)(qsrc + lane * 16), a1 = *(const u32x4*)(qsrc + 1280 + lane * 16);
    u32x2 t0 = *(const u32x2*)(qsrc + 1024 + r32 * 8), t1 = *(const u32x2*)(qsrc + 1280 + 1024 + r32 * 8);
    AT_STAGE(0); AT_STAGE(1); AT_STAGE(2); AT_STAGE(3);
    if (hi) { t0 = (u32x2){0u, 0u}; t1 = (u32x2){0u, 0u}; }
    qm0 = BF8(a0); qm1 = BF8(a1); qt0 = __builtin_bit_cast(s16x4, t0); qt1 = __builtin_bit_cast(s16x4, t1);
    PIN(qm0); PIN(qm1); PIN(qt0); PIN(qt1);
  }
  lds_ptr kp = smem + lane * 16, ktp = smem + 1024 + r32 * 8, vp = smem + 5120 + hi * 448 + (r32 < 28 ? r32 : 27) * 16;
  LAS int* flag = (LAS int*)(smem + AT_SLOTS * KV_STAGE);
  if (tid == 0) *flag = 0;
  WAIT_V(0); WAIT_L(0);
  BAR();
  f32x16 negm0, negm1;
  {
    float m0 = -3.0e38f, m1 = -3.0e38f;
#pragma unroll
    for (int kb = 0; kb < 2; ++kb) {
      const bf16x8 km = lds_frag(kp + kb * 1280); const s16x4 kt = *(LAS s16x4*)(ktp + kb * 1280);
      f32x16 s = MFMA32(km, qm0, splat16(0.f)); s = MFMA32T(kt, qt0, s); m0 = fmaxf(m0, max16(s));
      s = MFMA32(km, qm1, splat16(0.f)); s = MFMA32T(kt, qt1, s); m1 = fmaxf(m1, max16(s));
    }
    negm0 = splat16(-(xhalf_max(m0) + MARGIN)); negm1 = splat16(-(xhalf_max(m1) + MARGIN));
    PIN(negm0); PIN(negm1);
  }
  f32x16 o0, o1;
  TSTAMP(2, 1);
  for (int pass = 0;; ++pass) {
    o0 = splat16(0.f); o1 = splat16(0.f);
    f32x16 sA, sB;
    u32x4 pA0, pA1, pB0, pB1;
    bf16x8 kmE, kmO, v0E, v0O, v1E, v1O; s16x4 ktE, ktO;
    kmE = lds_frag(kp); ktE = *(LAS s16x4*)(ktp);
    v0E = lds_frag(vp); v1E = lds_frag(vp + 896);
    v1O = v1E;
    pB1 = (u32x4){0u, 0u, 0u, 0u};
    sA = MFMA32(kmE, qm0, negm0); sA = MFMA32T(ktE, qt0, sA);
#define GPRIO 1
#define YIELD 3
#define PINF(x) asm volatile("" : "+v"(x))
#define PRIO(n) __builtin_amdgcn_s_setprio(n)
#define SLOT(MF, X, SX, i, PW, j) do { MF; SBAR(); X; SBAR(); \
    float e0 = EX2(SX[i]), e1 = EX2(SX[i + 1]); if (YIELD == 2) { PINF(e0); PINF(e1); SBAR(); PRIO(0); SBAR(); } \
    float e2 = EX2(SX[i + 2]); if (YIELD == 3 || YIELD == 5) { PINF(e0); PINF(e1); PINF(e2); SBAR(); PRIO(0); SBAR(); } \
    float e3 = EX2(SX[i + 3]); if (YIELD == 4) { PINF(e0); PINF(e1); PINF(e2); PINF(e3); SBAR(); PRIO(0); SBAR(); } \
    if (YIELD == 5) { PINF(e3); SBAR(); PRIO(2); SBAR(); } \
    { unsigned c0_ = cvtpk(e0, e1); if (YIELD != 5) { PINF(c0_); SBAR(); PRIO(2); SBAR(); } PW[j] = c0_; PW[j + 1] = cvtpk(e2, e3); } PIN(PW); SBAR(); } while (0)
#define UNIT(SX, PX0, PX1, SY, KM, KT, QM, QT, NEG, OPREV, VPREV, PPREV, OCUR, V0, X1, X2, X3, X4) do { \
      SLOT(SY = MFMA32(KM, QM, NEG), X1, SX, 0, PX0, 0); \
      SLOT(OPREV = MFMA32(VPREV, BF8(PPREV), OPREV), X2, SX, 4, PX0, 2); \
      SLOT(SY = MFMA32T(KT, QT, SY), X3, SX, 8, PX1, 0); \
      SLOT(OCUR = MFMA32(V0, BF8(PX0), OCUR), X4, SX, 12, PX1, 2); } while (0)
#define BLOCK(KMC, KTC, KMN, KTN, V0C, V0N, V1C, V1P, NK, NV, XA2, XA3, XB3) do { \
      UNIT(sA, pA0, pA1, sB, KMC, KTC, qm1, qt1, negm1, o1, V1P, pB1, o0, V0C, \
           KMN = lds_frag(kp + (NK)), KTN = *(LAS s16x4*)(ktp + (NK)); XA2, XA3, WAIT_LGKM0()); \
      UNIT(sB, pB0, pB1, sA, KMN, KTN, qm0, qt0, negm0, o0, V1C, pA1, o1, V0C, \
           V0N = lds_frag(vp + (NV)), V1P = lds_frag(vp + (NV) + 896), XB3, WAIT_LGKM0()); } while (0)
#define NOP_ ((void)0)
#define STAGE4(S, SN, TNEXT) do { \
      BLOCK(kmE, ktE, kmO, ktO, v0E, v0O, v1E, v1O, (S) * KV_STAGE + 1280, (S) * KV_STAGE + 2 * 896, NOP_, NOP_, NOP_); \
      BLOCK(kmO, ktO, kmE, ktE, v0O, v0E, v1O, v1E, (S) * KV_STAGE + 2560, (S) * KV_STAGE + 4 * 896, NOP_, NOP_, NOP_); \
      BLOCK(kmE, ktE, kmO, ktO, v0E, v0O, v1E, v1O, (S) * KV_STAGE + 3840, (S) * KV_STAGE + 6 * 896, NOP_, NOP_, NOP_); \
        \
      WAIT_V(6); BAR(); m0set(ldsw + (S) * KV_STAGE); \
      { const char* gsrc_ = kvg + (size_t)((TNEXT) & 15) * KV_STAGE; \
      BLOCK(kmO, ktO, kmE, ktE, v0O, v0E, v1O, v1E, (SN) * KV_STAGE, (SN) * KV_STAGE, dmaoff<0>(gsrc_, voffl), dmaoff<1024>(gsrc_, voffl), dmaoff<2048>(gsrc_, voffl)); } } while (0)
    for (int t = 0; t < 16; t += 4) {
      STAGE4(0, 1, t + 4);
      STAGE4(1, 2, t + 5);
      STAGE4(2, 3, t + 6);
      STAGE4(3, 0, t + 7);
    }
    o1 = MFMA32(v1O, BF8(pB1), o1);
    WAIT_V(0);
    bool bad = false;
    if (hi) bad = !(o0[8] > 0.f && o0[8] < 1.0e36f) || !(o1[8] > 0.f && o1[8] < 1.0e36f);
#pragma unroll
    for (int r = 0; r < 12; ++r) bad |= !(fabsf(o0[r]) < 3.0e38f) || !(fabsf(o1[r]) < 3.0e38f);
    if (pass == 0 && __any(bad) && lane == 0) *flag = 1;
    WAIT_L(0);
    BAR();
    if (pass == 1 || *(volatile LAS int*)flag == 0) break;
    float m0 = -3.0e38f, m1 = -3.0e38f;
    for (int t = 0; t < 16; ++t) {
      WAIT_L(0); BAR();
      AT_STAGE(t);
      WAIT_V(0); BAR();
      const int sb = (t & 3) * KV_STAGE;
#pragma unroll
      for (int kb = 0; kb < 4; ++kb) {
        const bf16x8 km = lds_frag(kp + sb + kb * 1280); const s16x4 kt = *(LAS s16x4*)(ktp + sb + kb * 1280);
        f32x16 s = MFMA32(km, qm0, splat16(0.f)); s = MFMA32T(kt, qt0, s); m0 = fmaxf(m0, max16(s));
        s = MFMA32(km, qm1, splat16(0.f)); s = MFMA32T(kt, qt1, s); m1 = fmaxf(m1, max16(s));
      }
    }
    negm0 = splat16(-xhalf_max(m0)); negm1 = splat16(-xhalf_max(m1));
    WAIT_L(0); BAR();
    AT_STAGE(0); AT_STAGE(1); AT_STAGE(2); AT_STAGE(3);
    WAIT_V(0); BAR();
  }
  TSTAMP(2, 2);
#pragma unroll
  for (int u = 0; u < 2; ++u) {
    const f32x16& o = u ? o1 : o0;
    auto sw = __builtin_amdgcn_permlane32_swap(__float_as_uint(o[8]), __float_as_uint(o[8]), false, false);
    const float rl = 1.0f / __uint_as_float(sw[1]);
    const int pos = pb * 64 + u * 32 + r32;
    char* rowb = ws + WS_O + (size_t)(pos >> 4) * 20480 + (pos & 15) * 16;
#pragma unroll
    for (int qq = 0; qq < 3; ++qq) {
      const int qd = 2 * qq + hi;
      if (qd < 5) {
        const int cq = head * 5 + qd, c8 = cq >> 1;
        char* dst = rowb + (c8 >> 2) * 1024 + (c8 & 3) * 256 + (cq & 1) * 8;
        __hip_atomic_store((gu64*)dst, ((unsigned long long)cvtpk(o[4 * qq + 2] * rl, o[4 * qq + 3] * rl) << 32) | cvtpk(o[4 * qq] * rl, o[4 * qq + 1] * rl), RLX_AGENT);
      }
    }
  }
  WAIT_V(0); BAR();
  if (tid == 0) __hip_atomic_fetch_add((gu32*)(ws + WS_CNTO) + pb * 64, 1u, RLX_AGENT);
  TSTAMP(2, 3);
}

constexpr int GU_STAGE = 8192 + 16384, GU_SLOTS = 3, GU_LDS = GU_STAGE * GU_SLOTS;
#define PACE 0
#define GPACE() ((void)0)
DI void gateup_body(const uint16_t* __restrict__ wg, const uint16_t* __restrict__ wu, char* __restrict__ ws, lds_ptr smem, const int gb) {
  const int tid = threadIdx.x, lane = tid & 63, wid = __builtin_amdgcn_readfirstlane(tid >> 6);
  const int rb = 2 * ((gb >> 3) & 7) + (gb & 1), cj = 4 * (gb >> 6) + ((gb >> 1) & 3);
  char* ws_dbg = ws; (void)ws_dbg; TSTAMP(1, 0);
  const int c0 = cj * 96, win = c0 & ~63, uo = (c0 - win) >> 5;
  const char* asrc = ws + WS_XN + (size_t)(rb * 8 + 2 * wid) * 20480;
  const uint16_t* pw = (wid >> 1 ? wu : wg) + win + (wid & 1) * 64;
  const int kr = lane >> 3, ch = lane & 7;
  unsigned wl[4];
#pragma unroll
  for (int i = 0; i < 4; ++i) { const int k = 8 * i + kr; const int f = ((k >> 1) & 1) | (((k >> 3) & 1) << 1);
    wl[i] = (unsigned)(k * FF + (((ch >> 1) ^ f) * 16 + (ch & 1) * 8)) * 2u - 1024u * i; }
  const unsigned lds0 = lds_addr(smem), al = lane * 16, al2 = al + 20480 - 1024;
  const unsigned ldsA = lds0 + 2 * wid * 1024, ldsW = lds0 + 8192 + wid * 4096;
  auto stage = [&](int ks, int slot) {
    const char* a_ = asrc + ks * 1024; const uint16_t* wk_ = pw + (size_t)ks * 32 * FF;
    m0set(ldsA + slot * GU_STAGE); dmaoff<0>(a_, al); dmaoff<1024>(a_, al2);
    m0set(ldsW + slot * GU_STAGE); dmaoff<0>(wk_, wl[0]); dmaoff<1024>(wk_, wl[1]); dmaoff<2048>(wk_, wl[2]); dmaoff<3072>(wk_, wl[3]);
  };
  const int wp = wid & 1, wn = wid >> 1;
  const int g = lane >> 4, i16 = lane & 15, q = i16 >> 2, p = i16 & 3;
  const int fsw = ((q >> 1) & 1) | ((g & 1) << 1);
  int toff[3];
#pragma unroll
  for (int j = 0; j < 3; ++j) { const int s = 2 * uo + 3 * wn + j; toff[j] = 8192 + (s >> 2) * 4096 + (8 * g + q) * 128 + (((s & 3) ^ fsw) * 32) + 8 * p; }
  const int aoff = 4 * wp * 1024 + lane * 16;
  f32x4 acc[4][6];
#pragma unroll
  for (int i = 0; i < 4; ++i)
#pragma unroll
    for (int j = 0; j < 6; ++j) acc[i][j] = (f32x4){0.f, 0.f, 0.f, 0.f};
  stage(0, 0); stage(1, 1); stage(2, 2);
  bf16x8 aE[4], wE[6], aO[4], wO[6];
#define GU_RDW(W, sb, j) (W[j] = cat8(tr_read(sb + toff[(j) % 3] + ((j) / 3) * 8192), tr_read(sb + toff[(j) % 3] + ((j) / 3) * 8192 + 512)))
  WAIT_V(12); BAR();
#pragma unroll
  for (int i = 0; i < 4; ++i) aE[i] = lds_frag(smem + aoff + i * 1024);
#pragma unroll
  for (int j = 0; j < 6; ++j) GU_RDW(wE, smem, j);
  int slot = 0;
#define GU_STEP(AC, WC, AN, WN, ks) do { \
    const int sn_ = slot == 2 ? 0 : slot + 1; \
    WAIT_LGKM0(); WAIT_V(6); BAR();            \
    lds_ptr nb_ = smem + sn_ * GU_STAGE; \
    const int kn_ = (ks) + 3 < 20 ? (ks) + 3 : 19; \
    const char* a_ = asrc + kn_ * 1024; const uint16_t* wk_ = pw + (size_t)kn_ * 32 * FF; \
    _Pragma("unroll") for (int j = 0; j < 6; ++j) { \
      acc[0][j] = MFMA16(WC[j], AC[0], acc[0][j]); SBAR(); \
      if (j == 0) { m0set(ldsA + slot * GU_STAGE); dmaoff<0>(a_, al); } \
      if (j == 1) dmaoff<1024>(a_, al2); \
      if (j == 2) { m0set(ldsW + slot * GU_STAGE); dmaoff<0>(wk_, wl[0]); } \
      if (j == 3) dmaoff<1024>(wk_, wl[1]); \
      if (j == 4) dmaoff<2048>(wk_, wl[2]); \
      if (j == 5) dmaoff<3072>(wk_, wl[3]); \
      GPACE(); SBAR(); \
      acc[1][j] = MFMA16(WC[j], AC[1], acc[1][j]); SBAR(); \
      if (j < 4) AN[j] = lds_frag(nb_ + aoff + j * 1024); \
      GPACE(); SBAR(); \
      acc[2][j] = MFMA16(WC[j], AC[2], acc[2][j]); SBAR(); \
      GPACE(); SBAR(); \
      acc[3][j] = MFMA16(WC[j], AC[3], acc[3][j]); SBAR(); \
      GU_RDW(WN, nb_, j); \
      GPACE(); SBAR(); } \
    slot = sn_; } while (0)
  for (int ks = 0; ks < 20; ks += 2) {
    GU_STEP(aE, wE, aO, wO, ks);
    GU_STEP(aO, wO, aE, wE, ks + 1);
  }
  WAIT_V(0);
  TSTAMP(1, 1);
#pragma unroll
  for (int i = 0; i < 4; ++i) {
    const int rt = rb * 8 + 4 * wp + i;
#pragma unroll
    for (int j = 0; j < 3; ++j) {
      float h[4];
#pragma unroll
      for (int r = 0; r < 4; ++r) {
        const float gv = acc[i][j][r], uv = acc[i][j + 3][r];
        h[r] = gv * uv * __builtin_amdgcn_rcpf(1.f + __builtin_amdgcn_exp2f(-1.4426950408889634f * gv));
      }
      const int n = c0 + wn * 48 + j * 16 + 4 * g;
      char* dst = ws + WS_H + ((size_t)rt * 48 + (n >> 5)) * 1024 + (i16 + 16 * ((n >> 3) & 3)) * 16 + ((n >> 2) & 1) * 8;
      __hip_atomic_store((gu64*)dst, ((unsigned long long)cvtpk(h[2], h[3]) << 32) | cvtpk(h[0], h[1]), RLX_AGENT);
    }
  }
  WAIT_V(0); BAR();
  if (tid == 0) __hip_atomic_fetch_add((gu32*)(ws + WS_CNT) + rb * 64, 1u, RLX_AGENT);
  TSTAMP(1, 2);
}

constexpr int G2_STAGE = 12288 + 8192, G2_LDS = 3 * G2_STAGE;
template <int KS, int NST>
DI void gemm96x64(const char* __restrict__ act, const uint16_t* __restrict__ W, const int rt0, const int pn, lds_ptr smem, f32x4 (&acc)[3][2]) {
  const int tid = threadIdx.x, lane = tid & 63, wid = __builtin_amdgcn_readfirstlane(tid >> 6);
  const int kr = lane >> 3, ch = lane & 7;
  unsigned ao[3], wo_[2];
#pragma unroll
  for (int i = 0; i < 3; ++i) { const int id = 3 * wid + i; int rt = rt0 + (id >> 1); rt = rt < 128 ? rt : 127; ao[i] = (unsigned)((rt * KS + (id & 1)) * 1024 + lane * 16 - 1024 * i); }
#pragma unroll
  for (int i = 0; i < 2; ++i) { const int k = 8 * (2 * wid + i) + kr; const int f = ((k >> 1) & 1) | (((k >> 3) & 1) << 1);
    wo_[i] = (unsigned)(k * DM + pn * 64 + (((ch >> 1) ^ f) * 16 + (ch & 1) * 8)) * 2u - 1024u * i; }
  const unsigned lds0 = lds_addr(smem), ldsA = lds0 + 3 * wid * 1024, ldsW = lds0 + 12288 + 2 * wid * 1024;
  auto stage = [&](int st, int slot) {
    const char* a_ = act + st * 2048; const uint16_t* w_ = W + (size_t)st * 64 * DM;
    m0set(ldsA + slot * G2_STAGE); dmaoff<0>(a_, ao[0]); dmaoff<1024>(a_, ao[1]); dmaoff<2048>(a_, ao[2]);
    m0set(ldsW + slot * G2_STAGE); dmaoff<0>(w_, wo_[0]); dmaoff<1024>(w_, wo_[1]);
  };
  const int wp = wid & 1, wn = wid >> 1;
  const int g = lane >> 4, i16 = lane & 15, q = i16 >> 2, p = i16 & 3;
  const int fsw = ((q >> 1) & 1) | ((g & 1) << 1);
  int woff[2];
#pragma unroll
  for (int t = 0; t < 2; ++t) woff[t] = 12288 + (8 * g + q) * 128 + (((2 * wn + t) ^ fsw) * 32) + 8 * p;
  const int aoff = 3 * wp * 2048 + lane * 16;
  stage(0, 0); stage(1, 1); stage(2, 2);
  bf16x8 aE[3], wE[2], aO[3], wO[2];
#define G2_RDA(A, sb, kk, i) (A[i] = lds_frag(sb + aoff + (i) * 2048 + (kk) * 1024))
#define G2_RDW(W_, sb, kk, t) (W_[t] = cat8(tr_read(sb + woff[t] + (kk) * 4096), tr_read(sb + woff[t] + (kk) * 4096 + 512)))
  WAIT_V(10); BAR();
#pragma unroll
  for (int i = 0; i < 3; ++i) G2_RDA(aE, smem, 0, i);
#pragma unroll
  for (int t = 0; t < 2; ++t) G2_RDW(wE, smem, 0, t);
  int slot = 0;
  for (int st = 0; st < NST; ++st) {
    lds_ptr cb = smem + slot * G2_STAGE;
    const int sn = slot == 2 ? 0 : slot + 1;
    lds_ptr nb = smem + sn * G2_STAGE;
    acc[0][0] = MFMA16(wE[0], aE[0], acc[0][0]); SBAR(); G2_RDA(aO, cb, 1, 0); SBAR();
    acc[1][0] = MFMA16(wE[0], aE[1], acc[1][0]); SBAR(); G2_RDA(aO, cb, 1, 1); SBAR();
    acc[2][0] = MFMA16(wE[0], aE[2], acc[2][0]); SBAR(); G2_RDA(aO, cb, 1, 2); SBAR();
    acc[0][1] = MFMA16(wE[1], aE[0], acc[0][1]); SBAR(); G2_RDW(wO, cb, 1, 0); SBAR();
    acc[1][1] = MFMA16(wE[1], aE[1], acc[1][1]); SBAR(); G2_RDW(wO, cb, 1, 1); SBAR();
    acc[2][1] = MFMA16(wE[1], aE[2], acc[2][1]); SBAR();
    WAIT_LGKM0(); WAIT_V(5); BAR();
    const int sx = st + 3 < NST ? st + 3 : NST - 1;
    const char* a_ = act + sx * 2048; const uint16_t* w_ = W + (size_t)sx * 64 * DM;
    acc[0][0] = MFMA16(wO[0], aO[0], acc[0][0]); SBAR(); m0set(ldsA + slot * G2_STAGE); dmaoff<0>(a_, ao[0]); G2_RDA(aE, nb, 0, 0); SBAR();
    acc[1][0] = MFMA16(wO[0], aO[1], acc[1][0]); SBAR(); dmaoff<1024>(a_, ao[1]); G2_RDA(aE, nb, 0, 1); SBAR();
    acc[2][0] = MFMA16(wO[0], aO[2], acc[2][0]); SBAR(); dmaoff<2048>(a_, ao[2]); G2_RDA(aE, nb, 0, 2); SBAR();
    acc[0][1] = MFMA16(wO[1], aO[0], acc[0][1]); SBAR(); m0set(ldsW + slot * G2_STAGE); dmaoff<0>(w_, wo_[0]); G2_RDW(wE, nb, 0, 0); SBAR();
    acc[1][1] = MFMA16(wO[1], aO[1], acc[1][1]); SBAR(); dmaoff<1024>(w_, wo_[1]); G2_RDW(wE, nb, 0, 1); SBAR();
    acc[2][1] = MFMA16(wO[1], aO[2], acc[2][1]); SBAR();
    slot = sn;
  }
  WAIT_V(0);
}

DI void wait_counters(const char* cbase, const int lo, const int hi_, const unsigned want) {
  const int wid = __builtin_amdgcn_readfirstlane(threadIdx.x >> 6);
  if (wid == 0) {
    gu32* cnt = (gu32*)cbase;
    for (int i = lo; i <= hi_; ++i)
      for (unsigned spins = 0; spins < (1u << 20); ++spins) {
        if ((unsigned)__builtin_amdgcn_readfirstlane(__hip_atomic_load(cnt + i * 64, RLX_AGENT)) >= want) break;
        __builtin_amdgcn_s_sleep(2);
      }
    __builtin_amdgcn_fence(__ATOMIC_ACQUIRE, "agent");
    WAIT_V(0);
  }
  BAR();
}
DI void final_body(const uint16_t* __restrict__ x, const uint16_t* __restrict__ wo, const uint16_t* __restrict__ wd, char* __restrict__ ws, uint16_t* __restrict__ out, lds_ptr smem, const int gb) {
  const int tid = threadIdx.x, lane = tid & 63, wid = __builtin_amdgcn_readfirstlane(tid >> 6);
  const int i = gb >> 3, rbk = (gb & 7) + 8 * (i / 10), pn = i % 10;
  if (i >= 30 || rbk >= 22) return;
  char* ws_dbg = ws; (void)ws_dbg;
  const int rlast = 96 * rbk + 95 < 2047 ? 96 * rbk + 95 : 2047;
  wait_counters(ws + WS_CNT, (96 * rbk) >> 7, rlast >> 7, 16u);
  TSTAMP(4, 0);
  f32x4 accH[3][2], acc[3][2];
#pragma unroll
  for (int a = 0; a < 3; ++a) { accH[a][0] = (f32x4){0.f, 0.f, 0.f, 0.f}; accH[a][1] = accH[a][0]; acc[a][0] = accH[a][0]; acc[a][1] = accH[a][0]; }
  gemm96x64<48, 24>(ws + WS_H, wd, rbk * 6, pn, smem, accH);
  TSTAMP(4, 1);
  wait_counters(ws + WS_CNTO, (96 * rbk) >> 6, rlast >> 6, 8u);
  TSTAMP(4, 2);
  const int wp = wid & 1, wn = wid >> 1, g = lane >> 4, i16 = lane & 15;
  u32x2 xv[3][2];
#pragma unroll
  for (int a = 0; a < 3; ++a) {
    int rt = rbk * 6 + 3 * wp + a; rt = rt < 128 ? rt : 127;
#pragma unroll
    for (int j = 0; j < 2; ++j) xv[a][j] = *(const u32x2*)(x + (size_t)(rt * 16 + i16) * DM + pn * 64 + (2 * wn + j) * 16 + 4 * g);
  }
  gemm96x64<20, 10>(ws + WS_O, wo, rbk * 6, pn, smem, acc);
  TSTAMP(4, 3);
#pragma unroll
  for (int a = 0; a < 3; ++a) {
    const int rt = rbk * 6 + 3 * wp + a;
    if (rt < 128)
#pragma unroll
      for (int j = 0; j < 2; ++j) {
        const float xf[4] = {bf_lo(xv[a][j][0]), bf_hi(xv[a][j][0]), bf_lo(xv[a][j][1]), bf_hi(xv[a][j][1])};
        float r[4];
#pragma unroll
        for (int e = 0; e < 4; ++e) r[e] = bf_round(xf[e] + bf_round(acc[a][j][e])) + bf_round(accH[a][j][e]);
        *(u32x2*)(out + (size_t)(rt * 16 + i16) * DM + pn * 64 + (2 * wn + j) * 16 + 4 * g) = (u32x2){cvtpk(r[0], r[1]), cvtpk(r[2], r[3])};
      }
  }
  TSTAMP(4, 4);
}

constexpr int MAIN_LDS = GU_LDS > AT_LDS ? GU_LDS : AT_LDS;
__global__ __launch_bounds__(256, 2) void k_main(const uint16_t* __restrict__ x, const uint16_t* __restrict__ wo, const uint16_t* __restrict__ wg, const uint16_t* __restrict__ wu, const uint16_t* __restrict__ wd, char* __restrict__ ws, uint16_t* __restrict__ out) {
  extern __shared__ __attribute__((aligned(16))) char smem_[];
  lds_ptr smem = (lds_ptr)smem_;
  const int b = blockIdx.x;
  if (b < 256) {
    __builtin_amdgcn_s_setprio(2);
    attn_body(ws, smem, b);
  } else {
    __builtin_amdgcn_s_setprio(GPRIO);
    gateup_body(wg, wu, ws, smem, b - 256);
    final_body(x, wo, wd, ws, out, smem, b - 256);
  }
}

extern "C" void kernel_launch(void* const* d_in, const int* in_sizes, int n_in, void* d_out, int out_size, void* d_ws, size_t ws_size, hipStream_t stream) {
  const uint16_t* x = (const uint16_t*)d_in[0];
  const uint16_t* wq = (const uint16_t*)d_in[1];
  const uint16_t* wk = (const uint16_t*)d_in[2];
  const uint16_t* wv = (const uint16_t*)d_in[3];
  const uint16_t* wo = (const uint16_t*)d_in[4];
  const uint16_t* wg = (const uint16_t*)d_in[6];
  const uint16_t* wu = (const uint16_t*)d_in[7];
  const uint16_t* wd = (const uint16_t*)d_in[8];
  char* ws = (char*)d_ws;
  (void)hipFuncSetAttribute((const void*)k_pre, hipFuncAttributeMaxDynamicSharedMemorySize, PRE_LDS);
  (void)hipFuncSetAttribute((const void*)k_main, hipFuncAttributeMaxDynamicSharedMemorySize, MAIN_LDS);
  k_pre<<<256, 256, PRE_LDS, stream>>>(x, wq, wk, wv, ws);
  k_main<<<512, 256, MAIN_LDS, stream>>>(x, wo, wg, wu, wd, ws, (uint16_t*)d_out);
}
```

```cpp
#include <hip/hip_runtime.h>
#include <hip/hip_bf16.h>
#include <stdint.h>
#define NO_TOUCH
#define YIELD 6
#define APRIO 2
#define GPRIO0 1

typedef float f32x2 __attribute__((ext_vector_type(2)));
typedef float f32x4 __attribute__((ext_vector_type(4)));
typedef float f32x16 __attribute__((ext_vector_type(16)));
typedef __bf16 bf16x2 __attribute__((ext_vector_type(2)));
typedef __bf16 bf16x8 __attribute__((ext_vector_type(8)));
typedef short s16x4 __attribute__((ext_vector_type(4)));
typedef unsigned u32x2 __attribute__((ext_vector_type(2)));
typedef unsigned u32x4 __attribute__((ext_vector_type(4)));
#define LAS __attribute__((address_space(3)))
#define DI __device__ __forceinline__
typedef LAS char* lds_ptr;
#define GAS __attribute__((address_space(1)))
typedef GAS unsigned gu32;
typedef GAS unsigned long long gu64;
#define RLX_AGENT __ATOMIC_RELAXED, __HIP_MEMORY_SCOPE_AGENT

constexpr int SEQ = 2048, DM = 640, NH = 32, DH = 20, FF = 1536;
constexpr size_t WS_XN = 0;
constexpr size_t WS_KV = WS_XN + 128 * 20 * 1024;
constexpr size_t WS_H  = WS_KV + 16 * 12288;
constexpr size_t WS_O  = WS_H + 128 * 48 * 1024;
constexpr size_t WS_FLAG = WS_O + 128 * 20 * 1024;
constexpr size_t F_GEN = WS_FLAG, F_KV = WS_FLAG + 1024, F_H = WS_FLAG + 2048, F_O = WS_FLAG + 3072, F_XN = WS_FLAG + 4096;
constexpr size_t WS_END = WS_FLAG + 6144;
constexpr int KV_STAGE = 12288;
#define TSTAMP(kid, idx) do {} while (0)
#define TFLUSH(kid, n) do {} while (0)
#define TARG
#define TPASS

DI unsigned cvtpk(float a, float b) { f32x2 v = {a, b}; bf16x2 r = __builtin_convertvector(v, bf16x2); return __builtin_bit_cast(unsigned, r); }
DI float bf_round(float a) { return __uint_as_float(cvtpk(a, 0.f) << 16); }
DI float bf_lo(unsigned u) { return __uint_as_float(u << 16); }
DI float bf_hi(unsigned u) { return __uint_as_float(u & 0xffff0000u); }
DI void glds16(const void* g, lds_ptr l) { __builtin_amdgcn_global_load_lds((const unsigned*)g, (LAS unsigned*)l, 16, 0, 0); }
DI void dma16(const void* sbase, unsigned voff, unsigned lds) {
  asm volatile("s_mov_b32 m0, %2\n\ts_nop 0\n\tglobal_load_lds_dwordx4 %0, %1" :: "v"(voff), "s"(sbase), "s"(lds) : "memory");
}
DI void dma16x3(const void* sbase, unsigned voff, unsigned lds) {
  asm volatile("s_mov_b32 m0, %2\n\ts_nop 0\n\tglobal_load_lds_dwordx4 %0, %1\n\tglobal_load_lds_dwordx4 %0, %1 offset:1024\n\tglobal_load_lds_dwordx4 %0, %1 offset:2048" :: "v"(voff), "s"(sbase), "s"(lds) : "memory");
}
DI unsigned lds_addr(lds_ptr p) { return (unsigned)(size_t)p; }
#define PIN(x) asm volatile("" : "+v"(x))
DI void m0set(unsigned lds) { asm volatile("s_mov_b32 m0, %0\n\ts_nop 0" :: "s"(lds) : "memory"); }
template <int OFF> DI void dmaoff(const void* sbase, unsigned voff) { asm volatile("global_load_lds_dwordx4 %0, %1 offset:%2" :: "v"(voff), "s"(sbase), "i"(OFF) : "memory"); }
template <int OFF> DI void dmaoff_dev(const void* sbase, unsigned voff) { asm volatile("global_load_lds_dwordx4 %0, %1 offset:%2 sc1" :: "v"(voff), "s"(sbase), "i"(OFF) : "memory"); }
DI void store16_dev(void* sbase, unsigned voff, u32x4 v) { asm volatile("global_store_dwordx4 %0, %1, %2 sc1\n\ts_nop 1" :: "v"(voff), "v"(v), "s"(sbase) : "memory"); }
DI void store8_dev(void* p, u32x2 v) { __hip_atomic_store((gu64*)p, ((unsigned long long)v[1] << 32) | v[0], RLX_AGENT); }
DI void touch(const void* sbase, unsigned voff, unsigned& sink) { asm volatile("global_load_dword %0, %1, %2" : "+v"(sink) : "v"(voff), "s"(sbase) : "memory"); }
template <int OFF> DI void load16_dev(u32x4& d, const void* sbase, unsigned voff) { asm volatile("global_load_dwordx4 %0, %1, %2 offset:%3 sc1" : "=v"(d) : "v"(voff), "s"(sbase), "i"(OFF) : "memory"); }
template <int OFF> DI void load16_asm(u32x4& d, const void* sbase, unsigned voff) { asm volatile("global_load_dwordx4 %0, %1, %2 offset:%3" : "=v"(d) : "v"(voff), "s"(sbase), "i"(OFF) : "memory"); }
#define WAIT_LGKM0() __builtin_amdgcn_s_waitcnt(0xC07F)
DI void publish(char* word, unsigned tag) { __hip_atomic_store((gu32*)word, tag, RLX_AGENT); }
DI void poll_words(const char* base, int n, unsigned tag) {
  const int lane = threadIdx.x & 63;
  for (unsigned spins = 0; spins < (1u << 16); ++spins) {
    unsigned v = tag;
    if (lane < n) v = __hip_atomic_load((gu32*)base + lane, RLX_AGENT);
    if (__all(v == tag)) break;
    __builtin_amdgcn_s_sleep(1);
  }
}
DI void poll_256(const char* base, unsigned tag) {
  const unsigned voff = (threadIdx.x & 63) * 16;
  for (unsigned spins = 0; spins < (1u << 16); ++spins) {
    u32x4 v;
    asm volatile("global_load_dwordx4 %0, %1, %2 sc1\n\ts_waitcnt vmcnt(0)" : "=&v"(v) : "v"(voff), "s"(base) : "memory");
    if (__all(v[0] == tag && v[1] == tag && v[2] == tag && v[3] == tag)) break;
    __builtin_amdgcn_s_sleep(1);
  }
}
DI s16x4 tr_read(lds_ptr p) { return __builtin_bit_cast(s16x4, __builtin_amdgcn_ds_read_tr16_b64_v4i16((LAS s16x4*)p)); }
DI bf16x8 cat8(s16x4 a, s16x4 b) { return __builtin_bit_cast(bf16x8, __builtin_shufflevector(a, b, 0, 1, 2, 3, 4, 5, 6, 7)); }
DI bf16x8 lds_frag(lds_ptr p) { return __builtin_bit_cast(bf16x8, *(LAS u32x4*)p); }
#define MFMA16(a, b, c) __builtin_amdgcn_mfma_f32_16x16x32_bf16((a), (b), (c), 0, 0, 0)
#define MFMA32(a, b, c) __builtin_amdgcn_mfma_f32_32x32x16_bf16((a), (b), (c), 0, 0, 0)
#define MFMA32T(a, b, c) __builtin_amdgcn_mfma_f32_32x32x8bf16_1k((a), (b), (c), 0, 0, 0)
#define WAIT_V(n) asm volatile("s_waitcnt vmcnt(" #n ")" ::: "memory")
#define WAIT_L(n) asm volatile("s_waitcnt lgkmcnt(" #n ")" ::: "memory")
#define BAR() __builtin_amdgcn_s_barrier()
#define SBAR() __builtin_amdgcn_sched_barrier(0)
template <int CTRL> DI float dpp_add(float x) { return x + __uint_as_float(__builtin_amdgcn_update_dpp(0u, __float_as_uint(x), CTRL, 0xf, 0xf, true)); }
DI float row16_sum(float x) { x = dpp_add<0x128>(x); x = dpp_add<0x124>(x); x = dpp_add<0x122>(x); x = dpp_add<0x121>(x); return x; }

DI float xhalf_max(float m) { auto r = __builtin_amdgcn_permlane32_swap(__float_as_uint(m), __float_as_uint(m), false, false); return fmaxf(__uint_as_float(r[0]), __uint_as_float(r[1])); }
DI float max16(const f32x16& s) {
  float a = fmaxf(fmaxf(s[0], s[1]), s[2]), b = fmaxf(fmaxf(s[3], s[4]), s[5]);
  a = fmaxf(fmaxf(a, s[6]), s[7]); b = fmaxf(fmaxf(b, s[8]), s[9]); a = fmaxf(fmaxf(a, s[10]), s[11]); b = fmaxf(fmaxf(b, s[12]), s[13]);
  return fmaxf(fmaxf(a, s[14]), fmaxf(b, s[15]));
}
DI f32x16 splat16(float v) { f32x16 r;
#pragma unroll
  for (int i = 0; i < 16; ++i) r[i] = v;
  return r; }
#define EX2(x) __builtin_amdgcn_exp2f(x)
#define BF8(x) __builtin_bit_cast(bf16x8, x)

constexpr int AT_SLOTS = 4;
constexpr float MARGIN = 96.f;
constexpr float QSCALE = 0.22360679774997896f * 1.4426950408889634f;
constexpr int AT_QIMG = AT_SLOTS * KV_STAGE, AT_FLAG = AT_QIMG + 10240;
DI void attn_body(const uint16_t* __restrict__ wq, char* __restrict__ ws, lds_ptr smem, const int a TARG) {
  const int tid = threadIdx.x, lane = tid & 63, wid = __builtin_amdgcn_readfirstlane(tid >> 6);
  const int pb = (a & 7) + 8 * ((a >> 3) & 3), hg = a >> 5;
  const int head = hg * 4 + wid, r32 = lane & 31, hi = lane >> 5;
  char* ws_dbg = ws; (void)ws_dbg; TSTAMP(2, 0);
  const unsigned lds0 = lds_addr(smem);
  const char* kvg = ws + WS_KV + wid * 3072;
  const unsigned ldsw = lds0 + wid * 3072, voffl = lane * 16;
#define AT_STAGE(t) do { const char* g_ = kvg + (size_t)(t) * KV_STAGE; m0set(ldsw + ((t) & 3) * KV_STAGE); dmaoff_dev<0>(g_, voffl); dmaoff_dev<1024>(g_, voffl); dmaoff_dev<2048>(g_, voffl); } while (0)
  bf16x8 qm0, qm1; s16x4 qt0, qt1;
  unsigned tag;
  {
    const int g = lane >> 4, i16 = lane & 15, q = i16 >> 2, p = i16 & 3;
    const unsigned g0 = __hip_atomic_load((gu32*)(ws + F_GEN), RLX_AGENT);
    const int c0 = hg * 80, win = c0 & ~63, coff = c0 - win;
    const int kr = lane >> 3, ch = lane & 7;
    unsigned wl[2];
#pragma unroll
    for (int i = 0; i < 2; ++i) { const int k = 16 * (wid & 1) + 8 * i + kr; const int f = ((k >> 1) & 1) | (((k >> 3) & 1) << 1);
      wl[i] = (unsigned)(k * DM + (wid >> 1) * 64 + (((ch >> 1) ^ f) * 16 + (ch & 1) * 8)) * 2u - 1024u * i; }
    const uint16_t* wqw = wq + win;
#define AQ_STAGE(st) do { const uint16_t* s_ = wqw + (size_t)(st) * 32 * DM; m0set(lds0 + ((st) % 10) * 8192 + wid * 2048); dmaoff<0>(s_, wl[0]); dmaoff<1024>(s_, wl[1]); } while (0)
#define ASLEEP 60
    if (ASLEEP) __builtin_amdgcn_s_sleep(ASLEEP);
#pragma unroll
    for (int st = 0; st < 10; ++st) AQ_STAGE(st);
    unsigned sink = 0;
    { int li = wid * 64 + lane; li = li < 160 ? li : 159; touch(wqw, (unsigned)((320 + ((a >> 3) & 3) * 80 + (li >> 1)) * DM + (li & 1) * 64) * 2u, sink); }
    const int fsw = ((q >> 1) & 1) | ((g & 1) << 1);
    int toff[5];
#pragma unroll
    for (int t = 0; t < 5; ++t) { const int sg = (coff >> 4) + t; toff[t] = (sg >> 2) * 4096 + (8 * g + q) * 128 + (((sg & 3) ^ fsw) * 32) + 8 * p; }
#define AQ_RD(W_, st) do { _Pragma("unroll") for (int t = 0; t < 5; ++t) { lds_ptr wp_ = smem + ((st) % 10) * 8192 + toff[t]; W_[t] = cat8(tr_read(wp_), tr_read(wp_ + 512)); } } while (0)
#define AQ_RD1(W_, st, t) do { lds_ptr wp_ = smem + ((st) % 10) * 8192 + toff[t]; W_[t] = cat8(tr_read(wp_), tr_read(wp_ + 512)); } while (0)
    bf16x8 w0[5], w1[5], w2[5], w3[5], w4[5];
    WAIT_V(0); BAR();
    asm volatile("" :: "v"(sink));
    AQ_RD(w0, 0); AQ_RD(w1, 1); AQ_RD(w2, 2); AQ_RD(w3, 3); AQ_RD(w4, 4);
    WAIT_LGKM0();
#pragma unroll
    for (int t = 0; t < 5; ++t) { PIN(w0[t]); PIN(w1[t]); PIN(w2[t]); PIN(w3[t]); PIN(w4[t]); }
    BAR();
    AQ_STAGE(10); AQ_STAGE(11); AQ_STAGE(12); AQ_STAGE(13); AQ_STAGE(14);
    tag = __builtin_amdgcn_readfirstlane(g0) + 1u;
    poll_words(ws + F_XN + (pb * 4 + wid) * 16, 4, tag);
    TSTAMP(2, 5);
    u32x4 xv[20];
    const char* xt = ws + WS_XN + (size_t)(pb * 4 + wid) * 20480; const unsigned xo = lane * 16;
#define XLD(ks) load16_dev<((ks) & 3) * 1024>(xv[ks], xt + ((ks) >> 2) * 4096, xo)
    XLD(0); XLD(1); XLD(2); XLD(3); XLD(4); XLD(5); XLD(6); XLD(7); XLD(8); XLD(9);
    u32x4 kvf;
    load16_dev<0>(kvf, ws + F_KV, lane * 16);
    asm volatile("s_waitcnt vmcnt(1)" : "+v"(xv[0]), "+v"(xv[1]), "+v"(xv[2]), "+v"(xv[3]), "+v"(xv[4]), "+v"(xv[5]), "+v"(xv[6]), "+v"(xv[7]), "+v"(xv[8]), "+v"(xv[9]) :: "memory");
    TSTAMP(2, 1);
    f32x4 qa[5];
#pragma unroll
    for (int t = 0; t < 5; ++t) qa[t] = (f32x4){0.f, 0.f, 0.f, 0.f};
#define AQ_STEP(WC, WN, st, RDN, RST, DST) do { \
      const uint16_t* s_ = wqw + (size_t)(DST) * 32 * DM; \
      qa[0] = MFMA16(WC[0], BF8(xv[st]), qa[0]); SBAR(); if ((DST) > 0) { m0set(lds0 + ((DST) % 10) * 8192 + wid * 2048); dmaoff<0>(s_, wl[0]); } if (RDN) AQ_RD1(WN, RST, 0); SBAR(); \
      qa[1] = MFMA16(WC[1], BF8(xv[st]), qa[1]); SBAR(); if (RDN) AQ_RD1(WN, RST, 1); SBAR(); \
      qa[2] = MFMA16(WC[2], BF8(xv[st]), qa[2]); SBAR(); if ((DST) > 0) dmaoff<1024>(s_, wl[1]); if (RDN) AQ_RD1(WN, RST, 2); SBAR(); \
      qa[3] = MFMA16(WC[3], BF8(xv[st]), qa[3]); SBAR(); if (RDN) AQ_RD1(WN, RST, 3); SBAR(); \
      qa[4] = MFMA16(WC[4], BF8(xv[st]), qa[4]); SBAR(); if (RDN) AQ_RD1(WN, RST, 4); SBAR(); } while (0)
    TSTAMP(3, 0);
    AQ_STEP(w0, w0, 0, false, 0, 0); AQ_RD(w0, 5); SBAR();
    AQ_STEP(w1, w1, 1, false, 0, 0); AQ_RD(w1, 6); SBAR();
    AQ_STEP(w2, w2, 2, false, 0, 0); AQ_RD(w2, 7); SBAR();
    AQ_STEP(w3, w3, 3, false, 0, 0); AQ_RD(w3, 8); SBAR();
    AQ_STEP(w4, w4, 4, false, 0, 0); AQ_RD(w4, 9); SBAR();
    TSTAMP(3, 1);
    XLD(10); XLD(11); XLD(12); XLD(13); XLD(14);
    asm volatile("s_waitcnt vmcnt(5) lgkmcnt(0)" : "+v"(kvf) :: "memory");
    BAR();
    TSTAMP(3, 2);
    AQ_STEP(w0, w0, 5, false, 0, 15); AQ_RD(w0, 10); SBAR();
    AQ_STEP(w1, w1, 6, false, 0, 16); AQ_RD(w1, 11); SBAR();
    AQ_STEP(w2, w2, 7, false, 0, 17); AQ_RD(w2, 12); SBAR();
    AQ_STEP(w3, w3, 8, false, 0, 18); AQ_RD(w3, 13); SBAR();
    AQ_STEP(w4, w4, 9, false, 0, 19); AQ_RD(w4, 14); SBAR();
    XLD(15); XLD(16); XLD(17); XLD(18); XLD(19);
    TSTAMP(3, 3);
    if (wid == 0 && !__all(kvf[0] == tag && kvf[1] == tag && kvf[2] == tag && kvf[3] == tag)) poll_256(ws + F_KV, tag);
    asm volatile("s_waitcnt vmcnt(15) lgkmcnt(0)" : "+v"(xv[10]), "+v"(xv[11]), "+v"(xv[12]), "+v"(xv[13]), "+v"(xv[14]) :: "memory");
    BAR();
    TSTAMP(3, 4);
    AT_STAGE(0); SBAR();
    AQ_STEP(w0, w0, 10, false, 0, 0); AT_STAGE(1); SBAR();
    AQ_STEP(w1, w1, 11, false, 0, 0); AT_STAGE(2); SBAR();
    AQ_STEP(w2, w2, 12, false, 0, 0);
    AQ_STEP(w3, w3, 13, false, 0, 0);
    AQ_STEP(w4, w4, 14, false, 0, 0);
    TSTAMP(3, 5);
    asm volatile("s_waitcnt vmcnt(9)" : "+v"(xv[15]), "+v"(xv[16]), "+v"(xv[17]), "+v"(xv[18]), "+v"(xv[19]) :: "memory");
    BAR();
    TSTAMP(3, 6);
    AQ_RD(w0, 15); AQ_RD(w1, 16); AQ_RD(w2, 17); AQ_RD(w3, 18); AQ_RD(w4, 19); SBAR();
    AQ_STEP(w0, w0, 15, false, 0, 0); AQ_STEP(w1, w1, 16, false, 0, 0); AQ_STEP(w2, w2, 17, false, 0, 0); AQ_STEP(w3, w3, 18, false, 0, 0); AQ_STEP(w4, w4, 19, false, 0, 0);
#undef XLD
    WAIT_LGKM0(); BAR();
    TSTAMP(3, 7);
    AT_STAGE(3);
    const int pos = wid * 16 + i16;
#pragma unroll
    for (int t = 0; t < 5; ++t) {
      const int cq = 4 * t + g, hl = cq / 5, qd = cq - 5 * hl;
      lds_ptr blk = smem + AT_QIMG + (hl * 2 + (pos >> 5)) * 1280;
      lds_ptr dst = qd < 4 ? blk + ((pos & 31) + 32 * (qd & 1)) * 16 + (qd >> 1) * 8 : blk + 1024 + (pos & 31) * 8;
      *(LAS u32x2*)dst = (u32x2){cvtpk(qa[t][0] * QSCALE, qa[t][1] * QSCALE), cvtpk(qa[t][2] * QSCALE, qa[t][3] * QSCALE)};
    }
    if (tid == 0) *(LAS int*)(smem + AT_FLAG) = 0;
    WAIT_LGKM0(); BAR();
    lds_ptr qb = smem + AT_QIMG + wid * 2560;
    qm0 = lds_frag(qb + lane * 16); qm1 = lds_frag(qb + 1280 + lane * 16);
    u32x2 t0 = *(LAS u32x2*)(qb + 1024 + r32 * 8), t1 = *(LAS u32x2*)(qb + 1280 + 1024 + r32 * 8);
    if (hi) { t0 = (u32x2){0u, 0u}; t1 = (u32x2){0u, 0u}; }
    qt0 = __builtin_bit_cast(s16x4, t0); qt1 = __builtin_bit_cast(s16x4, t1);
    PIN(qm0); PIN(qm1); PIN(qt0); PIN(qt1);
  }
  lds_ptr kp = smem + lane * 16, ktp = smem + 1024 + r32 * 8, vp = smem + 5120 + hi * 448 + (r32 < 28 ? r32 : 27) * 16;
  LAS int* flag = (LAS int*)(smem + AT_FLAG);
  WAIT_V(0); BAR();
  TSTAMP(2, 2);
  f32x16 negm0, negm1;
  {
    float m0 = -3.0e38f, m1 = -3.0e38f;
#pragma unroll
    for (int kb = 0; kb < 2; ++kb) {
      const bf16x8 km = lds_frag(kp + kb * 1280); const s16x4 kt = *(LAS s16x4*)(ktp + kb * 1280);
      f32x16 s = MFMA32(km, qm0, splat16(0.f)); s = MFMA32T(kt, qt0, s); m0 = fmaxf(m0, max16(s));
      s = MFMA32(km, qm1, splat16(0.f)); s = MFMA32T(kt, qt1, s); m1 = fmaxf(m1, max16(s));
    }
    negm0 = splat16(-(xhalf_max(m0) + MARGIN)); negm1 = splat16(-(xhalf_max(m1) + MARGIN));
    PIN(negm0); PIN(negm1);
  }
  f32x16 o0, o1;
  for (int pass = 0;; ++pass) {
    o0 = splat16(0.f); o1 = splat16(0.f);
    f32x16 sA, sB;
    u32x4 pA0, pA1, pB0, pB1;
    bf16x8 kmE, kmO, v0E, v0O, v1E, v1O; s16x4 ktE, ktO;
    kmE = lds_frag(kp); ktE = *(LAS s16x4*)(ktp);
    v0E = lds_frag(vp); v1E = lds_frag(vp + 896);
    v1O = v1E;
    pB1 = (u32x4){0u, 0u, 0u, 0u};
    sA = MFMA32(kmE, qm0, negm0); sA = MFMA32T(ktE, qt0, sA);
#define PINF(x) asm volatile("" : "+v"(x))
#define PRIO(n) __builtin_amdgcn_s_setprio(n)
#define SLOT(MF, X, SX, i, PW, j) do { MF; SBAR(); X; SBAR(); \
    float e0 = EX2(SX[i]), e1 = EX2(SX[i + 1]); if (YIELD == 2) { PINF(e0); PINF(e1); SBAR(); PRIO(0); SBAR(); } \
    float e2 = EX2(SX[i + 2]); if (YIELD == 3 || YIELD == 5 || (YIELD == 6 && ((i) & 4))) { PINF(e0); PINF(e1); PINF(e2); SBAR(); PRIO(0); SBAR(); } \
    float e3 = EX2(SX[i + 3]); if (YIELD == 4) { PINF(e0); PINF(e1); PINF(e2); PINF(e3); SBAR(); PRIO(0); SBAR(); } \
    if (YIELD == 5) { PINF(e3); SBAR(); PRIO(2); SBAR(); } \
    { unsigned c0_ = cvtpk(e0, e1); if (YIELD != 5 && (YIELD != 6 || ((i) & 4))) { PINF(c0_); SBAR(); PRIO(2); SBAR(); } PW[j] = c0_; PW[j + 1] = cvtpk(e2, e3); } PIN(PW); SBAR(); } while (0)
#define UNIT(SX, PX0, PX1, SY, KM, KT, QM, QT, NEG, OPREV, VPREV, PPREV, OCUR, V0, X1, X2, X3, X4) do { \
      SLOT(SY = MFMA32(KM, QM, NEG), X1, SX, 0, PX0, 0); \
      SLOT(OPREV = MFMA32(VPREV, BF8(PPREV), OPREV), X2, SX, 4, PX0, 2); \
      SLOT(SY = MFMA32T(KT, QT, SY), X3, SX, 8, PX1, 0); \
      SLOT(OCUR = MFMA32(V0, BF8(PX0), OCUR), X4, SX, 12, PX1, 2); } while (0)
#define BLOCK(KMC, KTC, KMN, KTN, V0C, V0N, V1C, V1P, NK, NV, XA2, XA3, XB3) do { \
      UNIT(sA, pA0, pA1, sB, KMC, KTC, qm1, qt1, negm1, o1, V1P, pB1, o0, V0C, \
           KMN = lds_frag(kp + (NK)), KTN = *(LAS s16x4*)(ktp + (NK)); XA2, XA3, WAIT_LGKM0()); \
      UNIT(sB, pB0, pB1, sA, KMN, KTN, qm0, qt0, negm0, o0, V1C, pA1, o1, V0C, \
           V0N = lds_frag(vp + (NV)), V1P = lds_frag(vp + (NV) + 896), XB3, WAIT_LGKM0()); } while (0)
#define NOP_ ((void)0)
#define STAGE4(S, SN, TNEXT) do { \
      BLOCK(kmE, ktE, kmO, ktO, v0E, v0O, v1E, v1O, (S) * KV_STAGE + 1280, (S) * KV_STAGE + 2 * 896, NOP_, NOP_, NOP_); \
      BLOCK(kmO, ktO, kmE, ktE, v0O, v0E, v1O, v1E, (S) * KV_STAGE + 2560, (S) * KV_STAGE + 4 * 896, NOP_, NOP_, NOP_); \
      BLOCK(kmE, ktE, kmO, ktO, v0E, v0O, v1E, v1O, (S) * KV_STAGE + 3840, (S) * KV_STAGE + 6 * 896, NOP_, NOP_, NOP_); \
        \
      WAIT_V(6); BAR(); m0set(ldsw + (S) * KV_STAGE); \
      { const char* gsrc_ = kvg + (size_t)((TNEXT) & 15) * KV_STAGE; \
      BLOCK(kmO, ktO, kmE, ktE, v0O, v0E, v1O, v1E, (SN) * KV_STAGE, (SN) * KV_STAGE, dmaoff_dev<0>(gsrc_, voffl), dmaoff_dev<1024>(gsrc_, voffl), dmaoff_dev<2048>(gsrc_, voffl)); } } while (0)
    for (int t = 0; t < 16; t += 4) {
      STAGE4(0, 1, t + 4);
      STAGE4(1, 2, t + 5);
      STAGE4(2, 3, t + 6);
      STAGE4(3, 0, t + 7);
    }
    o1 = MFMA32(v1O, BF8(pB1), o1);
    WAIT_V(0);
    bool bad = false;
    if (hi) bad = !(o0[8] > 0.f && o0[8] < 1.0e36f) || !(o1[8] > 0.f && o1[8] < 1.0e36f);
#pragma unroll
    for (int r = 0; r < 12; ++r) bad |= !(fabsf(o0[r]) < 3.0e38f) || !(fabsf(o1[r]) < 3.0e38f);
    if (pass == 0 && __any(bad) && lane == 0) *flag = 1;
    WAIT_L(0);
    BAR();
    if (pass == 1 || *(volatile LAS int*)flag == 0) break;
    float m0 = -3.0e38f, m1 = -3.0e38f;
    for (int t = 0; t < 16; ++t) {
      WAIT_L(0); BAR();
      AT_STAGE(t);
      WAIT_V(0); BAR();
      const int sb = (t & 3) * KV_STAGE;
#pragma unroll
      for (int kb = 0; kb < 4; ++kb) {
        const bf16x8 km = lds_frag(kp + sb + kb * 1280); const s16x4 kt = *(LAS s16x4*)(ktp + sb + kb * 1280);
        f32x16 s = MFMA32(km, qm0, splat16(0.f)); s = MFMA32T(kt, qt0, s); m0 = fmaxf(m0, max16(s));
        s = MFMA32(km, qm1, splat16(0.f)); s = MFMA32T(kt, qt1, s); m1 = fmaxf(m1, max16(s));
      }
    }
    negm0 = splat16(-xhalf_max(m0)); negm1 = splat16(-xhalf_max(m1));
    WAIT_L(0); BAR();
    AT_STAGE(0); AT_STAGE(1); AT_STAGE(2); AT_STAGE(3);
    WAIT_V(0); BAR();
  }
  TSTAMP(2, 3);
#pragma unroll
  for (int u = 0; u < 2; ++u) {
    const f32x16& o = u ? o1 : o0;
    auto sw = __builtin_amdgcn_permlane32_swap(__float_as_uint(o[8]), __float_as_uint(o[8]), false, false);
    const float rl = 1.0f / __uint_as_float(sw[1]);
    const int pos = pb * 64 + u * 32 + r32;
    char* rowb = ws + WS_O + (size_t)(pos >> 4) * 20480 + (pos & 15) * 16;
#pragma unroll
    for (int qq = 0; qq < 3; ++qq) {
      const int qd = 2 * qq + hi;
      if (qd < 5) {
        const int cq = head * 5 + qd, c8 = cq >> 1;
        char* dst = rowb + (c8 >> 2) * 1024 + (c8 & 3) * 256 + (cq & 1) * 8;
        store8_dev(dst, (u32x2){cvtpk(o[4 * qq] * rl, o[4 * qq + 1] * rl), cvtpk(o[4 * qq + 2] * rl, o[4 * qq + 3] * rl)});
      }
    }
  }
  WAIT_V(0); BAR();
  if (tid == 0) publish(ws + F_O + (pb * 8 + hg) * 4, tag);
  TSTAMP(2, 4);
}

DI unsigned g_head(const uint16_t* __restrict__ x, const uint16_t* __restrict__ wk, const uint16_t* __restrict__ wv, const uint16_t* __restrict__ wg, const uint16_t* __restrict__ wu, char* __restrict__ ws, lds_ptr smem, const int gb TARG) {
  const int tid = threadIdx.x, lane = tid & 63, wid = __builtin_amdgcn_readfirstlane(tid >> 6);
  char* ws_dbg = ws; (void)ws_dbg; TSTAMP(0, 0);
  constexpr int XT = 51264;
  const unsigned lds0 = lds_addr(smem);
  const int c = lane & 15, rq = lane >> 4;
  u32x4 xv[5];
  unsigned sink = 0;
  if (wid < 2) {
    const uint16_t* xblk = x + (size_t)(gb * 8 + wid * 4) * DM;
    const unsigned xoff = (unsigned)(rq * DM + c * 8) * 2u;
    load16_asm<0>(xv[0], xblk, xoff); load16_asm<256>(xv[1], xblk, xoff); load16_asm<512>(xv[2], xblk, xoff); load16_asm<768>(xv[3], xblk, xoff); load16_asm<1024>(xv[4], xblk, xoff);
    asm volatile("s_waitcnt vmcnt(0)" : "+v"(xv[0]), "+v"(xv[1]), "+v"(xv[2]), "+v"(xv[3]), "+v"(xv[4]) :: "memory");
  } else {
    const uint16_t* wsel = wid == 2 ? wk : wv;
    for (int i = 0; i < 25; ++i) dma16(wsel + i * 512, lane * 16, lds0 + (wid - 2) * 25600 + i * 1024);
  }
  const unsigned g0 = __hip_atomic_load((gu32*)(ws + F_GEN), RLX_AGENT);
  if (wid < 2) {
    float f[40];
#pragma unroll
    for (int i = 0; i < 5; ++i)
#pragma unroll
      for (int j = 0; j < 4; ++j) { f[8 * i + 2 * j] = bf_lo(xv[i][j]); f[8 * i + 2 * j + 1] = bf_hi(xv[i][j]); }
    float s = 0.f;
#pragma unroll
    for (int i = 0; i < 40; ++i) s += f[i];
    const float mean = row16_sum(s) * (1.0f / DM);
    float v = 0.f;
#pragma unroll
    for (int i = 0; i < 40; ++i) { const float d = f[i] - mean; v = fmaf(d, d, v); }
    const float var = row16_sum(v) * (1.0f / DM);
    const float mr = bf_round(mean);
    const float sd = bf_round(__builtin_sqrtf(bf_round(bf_round(var) + 1e-5f)));
    const float rinv = 1.0f / sd;
    const int r8 = wid * 4 + rq;
    char* gx = ws + WS_XN + (size_t)(gb >> 1) * 20480;
#pragma unroll
    for (int i = 0; i < 5; ++i) {
      u32x4 o;
#pragma unroll
      for (int j = 0; j < 4; ++j) {
        const unsigned d2 = cvtpk(f[8 * i + 2 * j] - mr, f[8 * i + 2 * j + 1] - mr);
        o[j] = cvtpk(bf_lo(d2) * rinv, bf_hi(d2) * rinv);
      }
      const int off = (4 * i + (c >> 2)) * 1024 + 16 * (c & 3) * 16;
      store16_dev(gx, off + ((gb & 1) * 8 + r8) * 16, o);
      *(LAS u32x4*)(smem + XT + off + r8 * 16) = o;
    }
  }
  const unsigned tag = __builtin_amdgcn_readfirstlane(g0) + 1u;
  if (wid >= 2) WAIT_V(0);
  WAIT_LGKM0(); BAR();
  TSTAMP(0, 1);
  const int sel = wid >> 1, t = wid & 1;
  const int g = lane >> 4, i16 = lane & 15, q = i16 >> 2, p = i16 & 3;
  lds_ptr wbase = smem + sel * 25600 + (8 * g + q) * 40 + 32 * t + 8 * p;
  f32x4 acc0 = {0.f, 0.f, 0.f, 0.f}, acc1 = acc0;
#pragma unroll
  for (int k5 = 0; k5 < 2; ++k5) {
    bf16x8 a[10], w[10];
#pragma unroll
    for (int i = 0; i < 10; ++i) { const int ks = 10 * k5 + i; a[i] = lds_frag(smem + XT + ks * 1024 + lane * 16); w[i] = cat8(tr_read(wbase + ks * 1280), tr_read(wbase + ks * 1280 + 160)); }
    SBAR();
#pragma unroll
    for (int i = 0; i < 10; ++i) { if (i & 1) acc1 = sel == 0 ? MFMA16(w[i], a[i], acc1) : MFMA16(a[i], w[i], acc1); else acc0 = sel == 0 ? MFMA16(w[i], a[i], acc0) : MFMA16(a[i], w[i], acc0); }
  }
  PIN(acc0); PIN(acc1);
  if (wid < 2) {
    WAIT_V(0);
    if (lane == 0) publish(ws + F_XN + (gb * 2 + wid) * 4, tag);
  }
  const f32x4 acc = acc0 + acc1;
  const u32x2 pk = {cvtpk(acc[0], acc[1]), cvtpk(acc[2], acc[3])};
  if (sel == 0) {
    const int kb = gb >> 2, key = 8 * (gb & 3) + i16;
    char* blk = ws + WS_KV + (size_t)(kb >> 2) * KV_STAGE + (kb & 3) * 1280;
    if (i16 < 8) {
      if (t == 0) store8_dev(blk + (key + 32 * (g & 1)) * 16 + (g >> 1) * 8, pk);
      else if (g == 0) store8_dev(blk + 1024 + key * 8, pk);
    }
  } else if (g < 2) {
    const int d = 16 * t + i16, ks = gb >> 1, gg = 2 * (gb & 1) + g;
    char* blk = ws + WS_KV + (size_t)(ks >> 3) * KV_STAGE + 5120 + (ks & 7) * 896 + (gg & 1) * 448 + d * 16 + (gg >> 1) * 8;
    if (d < 20) store8_dev(blk, pk);
    else if (d < 28) { const unsigned cst = (d == 20) ? 0x3f803f80u : 0u; store8_dev(blk, (u32x2){cst, cst}); }
  }
  WAIT_V(0); WAIT_LGKM0(); BAR();
  asm volatile("" :: "v"(sink));
  if (tid == 0) publish(ws + F_KV + gb * 4, tag);
  TSTAMP(0, 2);
  return tag;
}

constexpr int GU_STAGE = 8192 + 16384, GU_SLOTS = 3, GU_LDS = GU_STAGE * GU_SLOTS;
#define PACE 0
#define GPACE() ((void)0)
DI void gateup_body(const uint16_t* __restrict__ wg, const uint16_t* __restrict__ wu, char* __restrict__ ws, lds_ptr smem, const int gb, const unsigned tag TARG) {
  const int tid = threadIdx.x, lane = tid & 63, wid = __builtin_amdgcn_readfirstlane(tid >> 6);
  const int rb = 2 * ((gb >> 3) & 7) + (gb & 1), cj = 4 * (gb >> 6) + ((gb >> 1) & 3);
  char* ws_dbg = ws; (void)ws_dbg; TSTAMP(1, 0);
  const int c0 = cj * 96, win = c0 & ~63, uo = (c0 - win) >> 5;
  const char* asrc = ws + WS_XN + (size_t)(rb * 8 + 2 * wid) * 20480;
  const uint16_t* pw = (wid >> 1 ? wu : wg) + win + (wid & 1) * 64;
  const int kr = lane >> 3, ch = lane & 7;
  unsigned wl[4];
#pragma unroll
  for (int i = 0; i < 4; ++i) { const int k = 8 * i + kr;
    const int f = ((k >> 1) & 1) | (((k >> 3) & 1) << 1);
    wl[i] = (unsigned)(k * FF + (((ch >> 1) ^ f) * 16 + (ch & 1) * 8)) * 2u - 1024u * i; }
  const unsigned lds0 = lds_addr(smem), al = lane * 16, al2 = al + 20480 - 1024;
  const unsigned ldsA = lds0 + 2 * wid * 1024, ldsW = lds0 + 8192 + wid * 4096;
  const int wp = wid & 1, wn = wid >> 1;
  const int g = lane >> 4, i16 = lane & 15, q = i16 >> 2, p = i16 & 3;
  const int fsw = ((q >> 1) & 1) | ((g & 1) << 1);
  int toff[3];
#pragma unroll
  for (int j = 0; j < 3; ++j) { const int s = 2 * uo + 3 * wn + j; toff[j] = 8192 + (s >> 2) * 4096 + (8 * g + q) * 128 + (((s & 3) ^ fsw) * 32) + 8 * p; }
  const int aoff = 4 * wp * 1024 + lane * 16;
  f32x4 acc[4][6];
#pragma unroll
  for (int i = 0; i < 4; ++i)
#pragma unroll
    for (int j = 0; j < 6; ++j) acc[i][j] = (f32x4){0.f, 0.f, 0.f, 0.f};
#pragma unroll
  for (int st = 0; st < 3; ++st) { const uint16_t* wk_ = pw + (size_t)st * 32 * FF;
    m0set(ldsW + st * GU_STAGE); dmaoff<0>(wk_, wl[0]); dmaoff<1024>(wk_, wl[1]); dmaoff<2048>(wk_, wl[2]); dmaoff<3072>(wk_, wl[3]); }
  if (wid == 0) poll_words(ws + F_XN + rb * 128, 32, tag);
  BAR();
#pragma unroll
  for (int st = 0; st < 3; ++st) { m0set(ldsA + st * GU_STAGE); dmaoff_dev<0>(asrc + st * 1024, al); dmaoff_dev<1024>(asrc + st * 1024, al2); }
  bf16x8 aE[4], wE[6], aO[4], wO[6];
#define GU_RDW(W, sb, j) (W[j] = cat8(tr_read(sb + toff[(j) % 3] + ((j) / 3) * 8192), tr_read(sb + toff[(j) % 3] + ((j) / 3) * 8192 + 512)))
  WAIT_V(0); BAR();
  TSTAMP(1, 3);
#pragma unroll
  for (int i = 0; i < 4; ++i) aE[i] = lds_frag(smem + aoff + i * 1024);
#pragma unroll
  for (int j = 0; j < 6; ++j) GU_RDW(wE, smem, j);
  int slot = 0;
#define GU_STEP(AC, WC, AN, WN, ks) do { \
    const int sn_ = slot == 2 ? 0 : slot + 1; \
    WAIT_LGKM0(); WAIT_V(6); BAR();            \
    lds_ptr nb_ = smem + sn_ * GU_STAGE; \
    const int kn_ = (ks) + 3 < 20 ? (ks) + 3 : 19; \
    const char* a_ = asrc + kn_ * 1024; const uint16_t* wk_ = pw + (size_t)kn_ * 32 * FF; \
    _Pragma("unroll") for (int j = 0; j < 6; ++j) { \
      acc[0][j] = MFMA16(WC[j], AC[0], acc[0][j]); SBAR(); \
      if (j == 0) { m0set(ldsA + slot * GU_STAGE); dmaoff_dev<0>(a_, al); } \
      if (j == 1) dmaoff_dev<1024>(a_, al2); \
      if (j == 2) { m0set(ldsW + slot * GU_STAGE); dmaoff<0>(wk_, wl[0]); } \
      if (j == 3) dmaoff<1024>(wk_, wl[1]); \
      if (j == 4) dmaoff<2048>(wk_, wl[2]); \
      if (j == 5) dmaoff<3072>(wk_, wl[3]); \
      GPACE(); SBAR(); \
      acc[1][j] = MFMA16(WC[j], AC[1], acc[1][j]); SBAR(); \
      if (j < 4) AN[j] = lds_frag(nb_ + aoff + j * 1024); \
      GPACE(); SBAR(); \
      acc[2][j] = MFMA16(WC[j], AC[2], acc[2][j]); SBAR(); \
      GPACE(); SBAR(); \
      acc[3][j] = MFMA16(WC[j], AC[3], acc[3][j]); SBAR(); \
      GU_RDW(WN, nb_, j); \
      GPACE(); SBAR(); } \
    slot = sn_; } while (0)
  for (int ks = 0; ks < 20; ks += 2) {
    GU_STEP(aE, wE, aO, wO, ks);
    GU_STEP(aO, wO, aE, wE, ks + 1);
  }
  WAIT_V(0);
  TSTAMP(1, 1);
#pragma unroll
  for (int i = 0; i < 4; ++i) {
    const int rt = rb * 8 + 4 * wp + i;
#pragma unroll
    for (int j = 0; j < 3; ++j) {
      float h[4];
#pragma unroll
      for (int r = 0; r < 4; ++r) {
        const float gv = acc[i][j][r], uv = acc[i][j + 3][r];
        h[r] = gv * uv * __builtin_amdgcn_rcpf(1.f + __builtin_amdgcn_exp2f(-1.4426950408889634f * gv));
      }
      const int n = c0 + wn * 48 + j * 16 + 4 * g;
      char* dst = ws + WS_H + ((size_t)rt * 48 + (n >> 5)) * 1024 + (i16 + 16 * ((n >> 3) & 3)) * 16 + ((n >> 2) & 1) * 8;
      store8_dev(dst, (u32x2){cvtpk(h[0], h[1]), cvtpk(h[2], h[3])});
    }
  }
  WAIT_V(0); BAR();
  if (tid == 0) publish(ws + F_H + (rb * 16 + cj) * 4, tag);
  TSTAMP(1, 2);
}

constexpr int G2_STAGE = 12288 + 8192, G2_LDS = 4 * G2_STAGE;
template <int KS, int NST>
DI void gemm96x64(const char* __restrict__ act, const uint16_t* __restrict__ W, const int rt0, const int pn, lds_ptr smem, f32x4 (&acc)[3][2], const char* flags, const int nflags, const unsigned tag) {
  const int tid = threadIdx.x, lane = tid & 63, wid = __builtin_amdgcn_readfirstlane(tid >> 6);
  const int kr = lane >> 3, ch = lane & 7;
  unsigned ao[3], wo_[2];
#pragma unroll
  for (int i = 0; i < 3; ++i) { const int id = 3 * wid + i; int rt = rt0 + (id >> 1); rt = rt < 128 ? rt : 127; ao[i] = (unsigned)((rt * KS + (id & 1)) * 1024 + lane * 16 - 1024 * i); }
#pragma unroll
  for (int i = 0; i < 2; ++i) { const int k = 8 * (2 * wid + i) + kr; const int f = ((k >> 1) & 1) | (((k >> 3) & 1) << 1);
    wo_[i] = (unsigned)(k * DM + pn * 64 + (((ch >> 1) ^ f) * 16 + (ch & 1) * 8)) * 2u - 1024u * i; }
  const unsigned lds0 = lds_addr(smem), ldsA = lds0 + 3 * wid * 1024, ldsW = lds0 + 12288 + 2 * wid * 1024;
  const int wp = wid & 1, wn = wid >> 1;
  const int g = lane >> 4, i16 = lane & 15, q = i16 >> 2, p = i16 & 3;
  const int fsw = ((q >> 1) & 1) | ((g & 1) << 1);
  int woff[2];
#pragma unroll
  for (int t = 0; t < 2; ++t) woff[t] = 12288 + (8 * g + q) * 128 + (((2 * wn + t) ^ fsw) * 32) + 8 * p;
  const int aoff = 3 * wp * 2048 + lane * 16;
#pragma unroll
  for (int st = 0; st < 4; ++st) { const uint16_t* w_ = W + (size_t)st * 64 * DM; m0set(ldsW + st * G2_STAGE); dmaoff<0>(w_, wo_[0]); dmaoff<1024>(w_, wo_[1]); }
  if (wid == 0) poll_words(flags, nflags, tag);
  BAR();
#pragma unroll
  for (int st = 0; st < 4; ++st) { const char* a_ = act + st * 2048; m0set(ldsA + st * G2_STAGE); dmaoff_dev<0>(a_, ao[0]); dmaoff_dev<1024>(a_, ao[1]); dmaoff_dev<2048>(a_, ao[2]); }
  bf16x8 aE[3], wE[2], aO[3], wO[2];
#define G2_RDA(A, sb, kk, i) (A[i] = lds_frag(sb + aoff + (i) * 2048 + (kk) * 1024))
#define G2_RDW(W_, sb, kk, t) (W_[t] = cat8(tr_read(sb + woff[t] + (kk) * 4096), tr_read(sb + woff[t] + (kk) * 4096 + 512)))
  WAIT_V(0); BAR();
#pragma unroll
  for (int i = 0; i < 3; ++i) G2_RDA(aE, smem, 0, i);
#pragma unroll
  for (int t = 0; t < 2; ++t) G2_RDW(wE, smem, 0, t);
  for (int st = 0; st < NST; ++st) {
    const int slot = st & 3;
    lds_ptr cb = smem + slot * G2_STAGE;
    lds_ptr nb = smem + ((st + 1) & 3) * G2_STAGE;
    acc[0][0] = MFMA16(wE[0], aE[0], acc[0][0]); SBAR(); G2_RDA(aO, cb, 1, 0); SBAR();
    acc[1][0] = MFMA16(wE[0], aE[1], acc[1][0]); SBAR(); G2_RDA(aO, cb, 1, 1); SBAR();
    acc[2][0] = MFMA16(wE[0], aE[2], acc[2][0]); SBAR(); G2_RDA(aO, cb, 1, 2); SBAR();
    acc[0][1] = MFMA16(wE[1], aE[0], acc[0][1]); SBAR(); G2_RDW(wO, cb, 1, 0); SBAR();
    acc[1][1] = MFMA16(wE[1], aE[1], acc[1][1]); SBAR(); G2_RDW(wO, cb, 1, 1); SBAR();
    acc[2][1] = MFMA16(wE[1], aE[2], acc[2][1]); SBAR();
    WAIT_LGKM0(); WAIT_V(10); BAR();
    const int sx = st + 4 < NST ? st + 4 : NST - 1;
    const char* a_ = act + sx * 2048; const uint16_t* w_ = W + (size_t)sx * 64 * DM;
    acc[0][0] = MFMA16(wO[0], aO[0], acc[0][0]); SBAR(); m0set(ldsA + slot * G2_STAGE); dmaoff_dev<0>(a_, ao[0]); G2_RDA(aE, nb, 0, 0); SBAR();
    acc[1][0] = MFMA16(wO[0], aO[1], acc[1][0]); SBAR(); dmaoff_dev<1024>(a_, ao[1]); G2_RDA(aE, nb, 0, 1); SBAR();
    acc[2][0] = MFMA16(wO[0], aO[2], acc[2][0]); SBAR(); dmaoff_dev<2048>(a_, ao[2]); G2_RDA(aE, nb, 0, 2); SBAR();
    acc[0][1] = MFMA16(wO[1], aO[0], acc[0][1]); SBAR(); m0set(ldsW + slot * G2_STAGE); dmaoff<0>(w_, wo_[0]); G2_RDW(wE, nb, 0, 0); SBAR();
    acc[1][1] = MFMA16(wO[1], aO[1], acc[1][1]); SBAR(); dmaoff<1024>(w_, wo_[1]); G2_RDW(wE, nb, 0, 1); SBAR();
    acc[2][1] = MFMA16(wO[1], aO[2], acc[2][1]); SBAR();
  }
  WAIT_V(0); WAIT_LGKM0(); BAR();
}

DI void final_body(const uint16_t* __restrict__ x, const uint16_t* __restrict__ wo, const uint16_t* __restrict__ wd, char* __restrict__ ws, uint16_t* __restrict__ out, lds_ptr smem, const int gb, const unsigned tag TARG) {
  const int tid = threadIdx.x, lane = tid & 63, wid = __builtin_amdgcn_readfirstlane(tid >> 6);
  const int i = gb >> 3, rbk = (gb & 7) + 8 * (i / 10), pn = i % 10;
  char* ws_dbg = ws; (void)ws_dbg;
  if (i >= 30 || rbk >= 22) {
    if (gb == 255 && wid == 0) {
      poll_256(ws + F_H, tag); poll_256(ws + F_O, tag);
      if (lane == 0) publish(ws + F_GEN, tag);
    }
    return;
  }
  const int rlast = 96 * rbk + 95 < 2047 ? 96 * rbk + 95 : 2047;
  TSTAMP(4, 0);
  f32x4 accH[3][2], acc[3][2];
#pragma unroll
  for (int a = 0; a < 3; ++a) { accH[a][0] = (f32x4){0.f, 0.f, 0.f, 0.f}; accH[a][1] = accH[a][0]; acc[a][0] = accH[a][0]; acc[a][1] = accH[a][0]; }
  { const int lo = (96 * rbk) >> 7, hi_ = rlast >> 7; gemm96x64<48, 24>(ws + WS_H, wd, rbk * 6, pn, smem, accH, ws + F_H + lo * 64, (hi_ - lo + 1) * 16, tag); }
  TSTAMP(4, 1);
  const int wp = wid & 1, wn = wid >> 1, g = lane >> 4, i16 = lane & 15;
  u32x2 xv[3][2];
#pragma unroll
  for (int a = 0; a < 3; ++a) {
    int rt = rbk * 6 + 3 * wp + a; rt = rt < 128 ? rt : 127;
#pragma unroll
    for (int j = 0; j < 2; ++j) xv[a][j] = *(const u32x2*)(x + (size_t)(rt * 16 + i16) * DM + pn * 64 + (2 * wn + j) * 16 + 4 * g);
  }
  { const int lo = (96 * rbk) >> 6, hi_ = rlast >> 6; gemm96x64<20, 10>(ws + WS_O, wo, rbk * 6, pn, smem, acc, ws + F_O + lo * 32, (hi_ - lo + 1) * 8, tag); }
  TSTAMP(4, 2);
#pragma unroll
  for (int a = 0; a < 3; ++a) {
    const int rt = rbk * 6 + 3 * wp + a;
    if (rt < 128)
#pragma unroll
      for (int j = 0; j < 2; ++j) {
        const float xf[4] = {bf_lo(xv[a][j][0]), bf_hi(xv[a][j][0]), bf_lo(xv[a][j][1]), bf_hi(xv[a][j][1])};
        float r[4];
#pragma unroll
        for (int e = 0; e < 4; ++e) r[e] = bf_round(xf[e] + bf_round(acc[a][j][e])) + bf_round(accH[a][j][e]);
        *(u32x2*)(out + (size_t)(rt * 16 + i16) * DM + pn * 64 + (2 * wn + j) * 16 + 4 * g) = (u32x2){cvtpk(r[0], r[1]), cvtpk(r[2], r[3])};
      }
  }
  TSTAMP(4, 3);
}

constexpr int MAIN_LDS = 81920;
static_assert(GU_LDS <= MAIN_LDS && G2_LDS <= MAIN_LDS && AT_FLAG + 16 <= MAIN_LDS, "LDS");
__global__ __launch_bounds__(256, 2) void k_mega(const uint16_t* __restrict__ x, const uint16_t* __restrict__ wq, const uint16_t* __restrict__ wk, const uint16_t* __restrict__ wv, const uint16_t* __restrict__ wo,
                                                 const uint16_t* __restrict__ wg, const uint16_t* __restrict__ wu, const uint16_t* __restrict__ wd, char* __restrict__ ws, uint16_t* __restrict__ out) {
  extern __shared__ __attribute__((aligned(16))) char smem_[];
  lds_ptr smem = (lds_ptr)smem_;
  const int b = blockIdx.x;
  if (b >= 256) {
    __builtin_amdgcn_s_setprio(APRIO);
    attn_body(wq, ws, smem, b - 256 TPASS);
    TFLUSH(2, 6); TFLUSH(3, 8);
  } else {
    __builtin_amdgcn_s_setprio(3);
    const unsigned tag = g_head(x, wk, wv, wg, wu, ws, smem, b TPASS);
    __builtin_amdgcn_s_setprio(GPRIO0);
    gateup_body(wg, wu, ws, smem, b, tag TPASS);
    final_body(x, wo, wd, ws, out, smem, b, tag TPASS);
    TFLUSH(0, 3); TFLUSH(1, 4); TFLUSH(4, 4);
  }
}

extern "C" void kernel_launch(void* const* d_in, const int* in_sizes, int n_in, void* d_out, int out_size, void* d_ws, size_t ws_size, hipStream_t stream) {
  (void)hipFuncSetAttribute((const void*)k_mega, hipFuncAttributeMaxDynamicSharedMemorySize, MAIN_LDS);
  k_mega<<<512, 256, MAIN_LDS, stream>>>((const uint16_t*)d_in[0], (const uint16_t*)d_in[1], (const uint16_t*)d_in[2], (const uint16_t*)d_in[3], (const uint16_t*)d_in[4],
                                         (const uint16_t*)d_in[6], (const uint16_t*)d_in[7], (const uint16_t*)d_in[8], (char*)d_ws, (uint16_t*)d_out);
}
```
